```python
import torch

def _softmax(x, axis=-1):
    return torch.softmax(x, dim=axis)
def _gelu(x):
    return torch.nn.functional.gelu(x, approximate="tanh")
def _silu(x):
    return torch.nn.functional.silu(x)
def _sigmoid(x):
    return torch.sigmoid(x)
def _layernorm(x, axis=-1, eps=1e-5):
    m = x.mean(dim=axis, keepdim=True)
    v = x.var(dim=axis, keepdim=True, unbiased=False)
    return (x - m) / torch.sqrt(v + eps)
def _scatter_add(tgt, idx, src, axis=0):
    return tgt.clone().index_add_(axis, idx, src)
def _segment_sum(src, seg, n):
    out = torch.zeros((n,) + src.shape[1:], dtype=src.dtype)
    return out.index_add_(0, seg, src)
def _pad(x, pw):
    flat = tuple(v for pair in reversed(pw) for v in pair)
    return torch.nn.functional.pad(x, flat)

kernel_name = 'gated_mix__ssm_conv_s4d__grouped_kv_mqa__prompt'

def setup_inputs():

    _w = (lambda a: (lambda t: t.to(torch.bfloat16) if t.is_floating_point() else t)(torch.as_tensor(a)))
    def _A_setup():
        Bsz, L, D, N = 1, 2048, 640, 128
        return {
            "u": _w((lambda *s: torch.randn(*s, dtype=torch.bfloat16) * 0.05)(Bsz, L, D)),
            "log_A_real": _w((lambda *s: torch.randn(*s, dtype=torch.bfloat16) * 6.4)(D, N) * 0.5),
            "A_imag": _w((lambda *s: torch.randn(*s, dtype=torch.bfloat16) * 0.05)(D, N) * 3.14),
            "C_real": _w((lambda *s: torch.randn(*s, dtype=torch.bfloat16) * 0.05)(D, N) / N ** 0.5),
            "C_imag": _w((lambda *s: torch.randn(*s, dtype=torch.bfloat16) * 0.05)(D, N) / N ** 0.5),
            "log_dt": _w((lambda *s: torch.randn(*s, dtype=torch.bfloat16) * 0.05)(D) * 0.5 - 4.0),
            "D_skip": _w((lambda *s: torch.randn(*s, dtype=torch.bfloat16) * 0.05)(D)),
        }
    _A_in = _A_setup()

    def _B_setup():
        B, S, D, H = 1, 2048, 640, 32
        Dh = D // H
        return {
            "x": _w((lambda *s: torch.randn(*s, dtype=torch.bfloat16) * 0.05)(B, S, D)),
            "wq": _w((lambda *s: torch.randn(*s, dtype=torch.bfloat16) * 0.8)(D, H * Dh)),
            "wk": _w((lambda *s: torch.randn(*s, dtype=torch.bfloat16) * 0.8)(D, Dh)),
            "wv": _w((lambda *s: torch.randn(*s, dtype=torch.bfloat16) * 0.05)(D, Dh)),
            "wo": _w((lambda *s: torch.randn(*s, dtype=torch.bfloat16) * 0.05)(H * Dh, D)),
        }
    _B_in = _B_setup()

    return {
        "A_u": _A_in["u"],
        "A_log_A_real": _A_in["log_A_real"],
        "A_A_imag": _A_in["A_imag"],
        "A_C_real": _A_in["C_real"],
        "A_C_imag": _A_in["C_imag"],
        "A_log_dt": _A_in["log_dt"],
        "A_D_skip": _A_in["D_skip"],
        "B_x": _B_in["x"],
        "B_wq": _B_in["wq"],
        "B_wk": _B_in["wk"],
        "B_wv": _B_in["wv"],
        "B_wo": _B_in["wo"],
        "gate_w": _w((lambda *s: torch.randn(*s, dtype=torch.bfloat16) * 0.05)(640) * 16.0),
    }

def reference(A_u, A_log_A_real, A_A_imag, A_C_real, A_C_imag, A_log_dt, A_D_skip, B_x, B_wq, B_wk, B_wv, B_wo, gate_w):
    def _A_ref(u, log_A_real, A_imag, C_real, C_imag, log_dt, D_skip):
        L = u.shape[1]
        dt = torch.exp(log_dt)
        A = -torch.exp(log_A_real) + 1j * A_imag
        C = (C_real + 1j * C_imag) * dt[:, None]
        pos = (lambda x, dt: x.to(dt))(torch.arange(L), log_dt.dtype)
        P = torch.exp((A * dt[:, None])[:, :, None] * pos[None, None, :])
        K = 2.0 * torch.einsum("dn,dnl->dl", C, P).real
        L2 = 2 * L
        u_t = (lambda x, *d: x.permute(*d))(u, 0, 2, 1)
        Kf = (lambda x, *a, **k: torch.fft.rfft(x.float(), *a, **k))(K, L2)
        Uf = (lambda x, *a, **k: torch.fft.rfft(x.float(), *a, **k))(u_t, L2)
        y = (lambda x, *a, **k: torch.fft.irfft(x, *a, **k).to(torch.bfloat16))(Uf * Kf[None, :, :], L2)[:, :, :L]
        y = (lambda x, *d: x.permute(*d))(y, 0, 2, 1)
        return y + u * D_skip[None, None, :]

    def _B_ref(x, wq, wk, wv, wo):
        B, S, D = x.shape
        H = 32
        Dh = D // H
        q = (lambda x, *d: x.permute(*d))((x @ wq).reshape(B, S, H, Dh), 0, 2, 1, 3)
        k = x @ wk
        v = x @ wv
        scores = torch.einsum("bhqd,bkd->bhqk", q, k) * (Dh ** -0.5)
        p = _softmax(scores, axis=-1)
        o = torch.einsum("bhqk,bkd->bhqd", p, v)
        return (lambda x, *d: x.permute(*d))(o, 0, 2, 1, 3).reshape(B, S, H * Dh) @ wo

    x = A_u
    g = _sigmoid((x * gate_w).sum(-1, keepdims=True))
    a = _A_ref(x, A_log_A_real, A_A_imag, A_C_real, A_C_imag, A_log_dt, A_D_skip)
    b = _B_ref(x, B_wq, B_wk, B_wv, B_wo)
    return g * a + (1 - g) * b
```

```cpp
#include <hip/hip_runtime.h>
#include <stdint.h>
#include <type_traits>

#define LAS __attribute__((address_space(3)))
#define GAS __attribute__((address_space(1)))
#define DEV __device__ __forceinline__
typedef unsigned short u16;
typedef unsigned long long u64;
typedef __attribute__((ext_vector_type(2))) unsigned u32x2;
typedef __attribute__((ext_vector_type(4))) unsigned u32x4;
typedef __attribute__((ext_vector_type(2))) float f32x2;
typedef __attribute__((ext_vector_type(4))) float f32x4;
typedef __attribute__((ext_vector_type(8))) float f32x8;
typedef __attribute__((ext_vector_type(16))) float f32x16;
typedef __attribute__((ext_vector_type(2))) __bf16 bf16x2;
typedef __attribute__((ext_vector_type(4))) __bf16 bf16x4;
typedef __attribute__((ext_vector_type(8))) __bf16 bf16x8;
typedef __attribute__((ext_vector_type(16))) __bf16 bf16x16;
typedef __attribute__((ext_vector_type(2))) _Float16 f16x2;
typedef __attribute__((ext_vector_type(8))) _Float16 f16x8;
typedef __attribute__((ext_vector_type(4))) short s16x4;
typedef LAS char* lptr;

constexpr int SEQ = 2048, DM = 640, ROWB = DM * 2;

constexpr unsigned WS_EPOCH = 0;
constexpr unsigned FL = 128;
constexpr unsigned WS_ARR = 4096;
constexpr unsigned WS_UT = 524288;
constexpr unsigned WS_AT = WS_UT + 2621440;
constexpr unsigned WS_OF = WS_AT + 2621440;
constexpr unsigned WS_END = WS_OF + 2621440;
constexpr unsigned WS_FKV = WS_END;
constexpr unsigned WS_FA = WS_FKV + 512 * FL;
constexpr unsigned WS_LAST = WS_FA + 2048 * FL;
constexpr unsigned WS_FUT = 8192 + 128 * FL;
constexpr unsigned WS_FO = WS_FUT + 512 * FL;
constexpr unsigned WS_KV = WS_FO + 1024 * FL;
static_assert(WS_KV + 8 * 20992 <= WS_UT, "ws");

constexpr unsigned REC = 2368, V1_OFF = 672, QUAD_OFF = 8 * REC, CHUNK = QUAD_OFF + 2048;
constexpr unsigned LDS_BYTES = 163840;
constexpr unsigned KC_STRIDE = 4192;
constexpr unsigned KC_W = 17408;
constexpr unsigned UCOL = 272;
constexpr unsigned L_KC = 512;
constexpr unsigned L_QST = L_KC + 4 * KC_W;
constexpr unsigned QSLOT = 18432;
constexpr unsigned L_GW = L_QST + 5 * QSLOT;
constexpr unsigned L_UP = L_QST + 512;
constexpr unsigned L_WO = L_KC;
constexpr unsigned L_ATILE = L_WO + 102400;
constexpr unsigned L_RING = LDS_BYTES - 2 * CHUNK;
constexpr unsigned L_FIRST = L_RING - 2048;
static_assert(L_GW + 1280 <= LDS_BYTES, "lds");
static_assert(L_UP + 95 * UCOL <= L_ATILE , "lds");
constexpr unsigned M_KVRD = 0, M_CNT_S = 4, M_CNT_S2 = 8, M_KVRDY = 12, M_KMAX = 16, M_CNT_L = 20, M_WO = 24, M_KVISS = 28, M_ST = 32;
constexpr unsigned L_Z = L_ATILE + 10240;
constexpr unsigned L_QX = L_ATILE;
constexpr unsigned M_URDY = 56, M_CNT_U = 52, M_CNT_Q = 48, M_RAW = 64;
constexpr unsigned M_YRDY = 128;
constexpr unsigned M_PRG = 112;
static_assert(L_Z + 256 <= L_FIRST, "lds");

constexpr unsigned SPIN_CAP = 1u << 18;
#define POLL_AT 0
constexpr float LOG2E = 1.4426950408889634f;

constexpr u64 cl_tab() {
  u64 t = 0;
  for (int bi = 0; bi < 32; ++bi) t |= (u64)(bi < 8 ? bi >> 1 : bi < 28 ? (bi - 8) / 5 : bi - 28) << (2 * bi);
  return t;
}
constexpr u64 j_tab(int hi) {
  u64 t = 0;
  for (int k = 0; k < 16; ++k) { const int bi = 16 * hi + k; t |= (u64)(bi < 8 ? 5 + (bi & 1) : bi < 28 ? (bi - 8) % 5 : 7) << (4 * k); }
  return t;
}
constexpr u64 CL_TAB = cl_tab(), J_LO = j_tab(0), J_HI = j_tab(1);

constexpr unsigned CODE_BEFORE = 0, CODE_AFTER = 49024;
#define PFW 64

struct Params {
  const u16 *x, *logA, *Aim, *Cre, *Cim, *logdt, *Dsk, *wq, *wk, *wv, *wo, *gw;
  u16* out;
  char* ws;
};

DEV float bf2f(unsigned v) { return __builtin_bit_cast(float, v << 16); }
DEV float bf_hi(unsigned v) { return __builtin_bit_cast(float, v & 0xffff0000u); }
DEV unsigned pk_bf(float a, float b) { f32x2 t = {a, b}; return __builtin_bit_cast(unsigned, __builtin_convertvector(t, bf16x2)); }
DEV unsigned pk_h(float a, float b) { f32x2 t = {a, b}; return __builtin_bit_cast(unsigned, __builtin_convertvector(t, f16x2)); }
DEV f16x2 pk2h(float a, float b) { f32x2 t = {a, b}; return __builtin_convertvector(t, f16x2); }
DEV u32x4 cat4(const f16x2 (&v)[4]) {
  return u32x4{__builtin_bit_cast(unsigned, v[0]), __builtin_bit_cast(unsigned, v[1]), __builtin_bit_cast(unsigned, v[2]), __builtin_bit_cast(unsigned, v[3])};
}
DEV float rbf(float a) { return bf2f(pk_bf(a, a) & 0xffffu); }
DEV unsigned opaque(unsigned v) { asm volatile("" : "+v"(v)); return v; }
DEV float dot2bf(unsigned a, unsigned b, float c) {
  return __builtin_amdgcn_fdot2_f32_bf16(__builtin_bit_cast(bf16x2, a), __builtin_bit_cast(bf16x2, b), c, false);
}
DEV unsigned rfl(unsigned v) { return __builtin_amdgcn_readfirstlane(v); }
DEV float xhalf(float v) {
  auto r = __builtin_amdgcn_permlane32_swap(__builtin_bit_cast(unsigned, v), __builtin_bit_cast(unsigned, v), false, false);
  const unsigned r0 = r[0], r1 = r[1];
  const unsigned t = (threadIdx.x & 32) ? r0 : r1;
  return __builtin_bit_cast(float, t);
}
DEV float wave_max(float f) {
  unsigned v = __builtin_bit_cast(unsigned, f);
  v = max(v, (unsigned)__builtin_amdgcn_update_dpp(0, (int)v, 0x111, 0xf, 0xf, false));
  v = max(v, (unsigned)__builtin_amdgcn_update_dpp(0, (int)v, 0x112, 0xf, 0xf, false));
  v = max(v, (unsigned)__builtin_amdgcn_update_dpp(0, (int)v, 0x114, 0xf, 0xf, false));
  v = max(v, (unsigned)__builtin_amdgcn_update_dpp(0, (int)v, 0x118, 0xf, 0xf, false));
  v = max(v, (unsigned)__builtin_amdgcn_update_dpp(0, (int)v, 0x142, 0xa, 0xf, false));
  v = max(v, (unsigned)__builtin_amdgcn_update_dpp(0, (int)v, 0x143, 0xc, 0xf, false));
  return __builtin_bit_cast(float, (unsigned)__builtin_amdgcn_readlane((int)v, 63));
}
DEV void touch(const void* base, unsigned off) {
  asm volatile("s_mov_b32 m0, 0x100\n\ts_nop 0\n\tglobal_load_lds_dword %0, %1" ::"v"(off), "s"(base) : "memory");
}
template <int N> DEV void wait_vm() { asm volatile("s_waitcnt vmcnt(%0)" ::"i"(N) : "memory"); }
DEV void wait_lgkm0() { asm volatile("s_waitcnt lgkmcnt(0)" ::: "memory"); }
DEV void hw_barrier() { asm volatile("s_waitcnt lgkmcnt(0)\n\ts_barrier" ::: "memory"); }

template <int OFF, bool SC1> DEV void dma16(const void* base, unsigned voff, unsigned lds) {
  if constexpr (SC1)
    asm volatile("s_mov_b32 m0, %2\n\ts_nop 0\n\tglobal_load_lds_dwordx4 %0, %1 offset:%3 sc1" ::"v"(voff), "s"(base), "s"(lds), "i"(OFF) : "memory");
  else
    asm volatile("s_mov_b32 m0, %2\n\ts_nop 0\n\tglobal_load_lds_dwordx4 %0, %1 offset:%3" ::"v"(voff), "s"(base), "s"(lds), "i"(OFF) : "memory");
}
template <bool SC1> DEV void dma_run(const void* base, unsigned voff, unsigned lds) {
  asm volatile("s_mov_b32 m0, %2\n\ts_nop 0\n\tglobal_load_lds_dwordx4 %0, %1 sc1\n\tglobal_load_lds_dwordx4 %0, %1 offset:1024 sc1\n\t"
               "global_load_lds_dwordx4 %0, %1 offset:2048 sc1\n\tglobal_load_lds_dwordx4 %0, %1 offset:3072 sc1" ::"v"(voff), "s"(base), "s"(lds) : "memory");
}
DEV void dma_slot(__amdgpu_buffer_rsrc_t xr, __amdgpu_buffer_rsrc_t wr, const unsigned (&xo)[4], const unsigned (&wo)[5], unsigned sx,
                  unsigned sq, unsigned sl) {
  unsigned sx2, sq2;
  asm volatile(
      "s_mov_b32 m0, %[sl]\n\ts_add_u32 %[sx2], %[sx], 0xa000\n\t"
      "buffer_load_dwordx4 %[x0], %[xr], %[sx] offen lds\n\tbuffer_load_dwordx4 %[x1], %[xr], %[sx] offen offset:1024 lds\n\t"
      "buffer_load_dwordx4 %[x2], %[xr], %[sx] offen offset:2048 lds\n\tbuffer_load_dwordx4 %[x3], %[xr], %[sx] offen offset:3072 lds\n\t"
      "s_add_u32 m0, %[sl], 0x1000\n\ts_add_u32 %[sq2], %[sq], 0xa000\n\t"
      "buffer_load_dwordx4 %[x0], %[xr], %[sx2] offen lds\n\tbuffer_load_dwordx4 %[x1], %[xr], %[sx2] offen offset:1024 lds\n\t"
      "buffer_load_dwordx4 %[x2], %[xr], %[sx2] offen offset:2048 lds\n\tbuffer_load_dwordx4 %[x3], %[xr], %[sx2] offen offset:3072 lds\n\t"
      "s_add_u32 m0, %[sl], 0x2000\n\ts_nop 0\n\t"
      "buffer_load_dwordx4 %[w0], %[wr], %[sq] offen lds\n\tbuffer_load_dwordx4 %[w1], %[wr], %[sq] offen offset:1024 lds\n\t"
      "buffer_load_dwordx4 %[w2], %[wr], %[sq] offen offset:2048 lds\n\t"
      "s_add_u32 m0, %[sl], 0x2c00\n\ts_nop 0\n\t"
      "buffer_load_dwordx4 %[w3], %[wr], %[sq] offen lds\n\tbuffer_load_dwordx4 %[w4], %[wr], %[sq] offen offset:1024 lds\n\t"
      "s_add_u32 m0, %[sl], 0x3400\n\ts_nop 0\n\t"
      "buffer_load_dwordx4 %[w0], %[wr], %[sq2] offen lds\n\tbuffer_load_dwordx4 %[w1], %[wr], %[sq2] offen offset:1024 lds\n\t"
      "buffer_load_dwordx4 %[w2], %[wr], %[sq2] offen offset:2048 lds\n\t"
      "s_add_u32 m0, %[sl], 0x4000\n\ts_nop 0\n\t"
      "buffer_load_dwordx4 %[w3], %[wr], %[sq2] offen lds\n\tbuffer_load_dwordx4 %[w4], %[wr], %[sq2] offen offset:1024 lds"
      : [sx2] "=&s"(sx2), [sq2] "=&s"(sq2)
      : [xr] "s"(xr), [wr] "s"(wr), [x0] "v"(xo[0]), [x1] "v"(xo[1]), [x2] "v"(xo[2]), [x3] "v"(xo[3]), [w0] "v"(wo[0]), [w1] "v"(wo[1]),
        [w2] "v"(wo[2]), [w3] "v"(wo[3]), [w4] "v"(wo[4]), [sx] "s"(sx), [sq] "s"(sq), [sl] "s"(sl)
      : "scc", "memory");
}
DEV void dma_mark(__amdgpu_buffer_rsrc_t rs, unsigned oob, unsigned lds) {
  asm volatile("s_mov_b32 m0, %2\n\ts_mov_b64 exec, 1\n\tbuffer_load_dword %0, %1, 0 offen lds\n\ts_mov_b64 exec, -1" ::"v"(oob), "s"(rs), "s"(lds) : "memory");
}
DEV u32x4 lds128(lptr s, unsigned off) { return *(LAS u32x4*)(s + off); }
DEV u32x2 lds64(lptr s, unsigned off) { return *(LAS u32x2*)(s + off); }
DEV u32x2 lds_tr(lptr s, unsigned off) { return __builtin_bit_cast(u32x2, __builtin_amdgcn_ds_read_tr16_b64_v4i16((LAS s16x4*)(s + off))); }
DEV u32x4 cat(u32x2 a, u32x2 b) { return u32x4{a.x, a.y, b.x, b.y}; }
DEV f32x16 mfma32(u32x4 a, u32x4 b, f32x16 c) {
  return __builtin_amdgcn_mfma_f32_32x32x16_bf16(__builtin_bit_cast(bf16x8, a), __builtin_bit_cast(bf16x8, b), c, 0, 0, 0);
}
DEV f32x16 mfma32h(u32x4 a, u32x4 b, f32x16 c) {
  return __builtin_amdgcn_mfma_f32_32x32x16_f16(__builtin_bit_cast(f16x8, a), __builtin_bit_cast(f16x8, b), c, 0, 0, 0);
}
DEV f32x4 mfma16(u32x4 a, u32x4 b, f32x4 c) {
  return __builtin_amdgcn_mfma_f32_16x16x32_bf16(__builtin_bit_cast(bf16x8, a), __builtin_bit_cast(bf16x8, b), c, 0, 0, 0);
}
DEV f32x16 zero16() { f32x16 z; for (int i = 0; i < 16; ++i) z[i] = 0.f; return z; }

DEV u64 gld64(const char* p) { return __hip_atomic_load((GAS u64*)p, __ATOMIC_RELAXED, __HIP_MEMORY_SCOPE_AGENT); }
DEV void gst64(char* p, u64 v) { __hip_atomic_store((GAS u64*)p, v, __ATOMIC_RELAXED, __HIP_MEMORY_SCOPE_AGENT); }
DEV unsigned lds_ld(lptr s, unsigned off) { return __hip_atomic_load((LAS unsigned*)(s + off), __ATOMIC_RELAXED, __HIP_MEMORY_SCOPE_WORKGROUP); }
DEV void lds_st(lptr s, unsigned off, unsigned v) { __hip_atomic_store((LAS unsigned*)(s + off), v, __ATOMIC_RELAXED, __HIP_MEMORY_SCOPE_WORKGROUP); }

DEV void lds_wait_zero(lptr s, unsigned off) {
#pragma unroll 1
  for (unsigned n = 0; n < SPIN_CAP * 8; ++n)
    if (rfl(lds_ld(s, off)) == 0) break;
  asm volatile("" ::: "memory");
}
DEV unsigned prog_min(lptr s) {
  const u32x4 v = *(volatile LAS u32x4*)(s + M_PRG);
  return min(min(v.x, v.y), v.z) - 1;
}
template <bool SLEEP> DEV void wait_prog(lptr s, unsigned slots) {
#pragma unroll 1
  for (unsigned n = 0; n < SPIN_CAP * 8; ++n) {
    if (rfl(prog_min(s)) >= slots) break;
    if (SLEEP) __builtin_amdgcn_s_sleep(1);
  }
  asm volatile("" ::: "memory");
}
template <bool SLEEP> DEV void lds_wait_ge(lptr s, unsigned off, unsigned target) {
#pragma unroll 1
  for (unsigned n = 0; n < SPIN_CAP * 8; ++n) {
    if (rfl(lds_ld(s, off)) >= target) break;
    if (SLEEP) __builtin_amdgcn_s_sleep(1);
  }
  asm volatile("" ::: "memory");
}
template <unsigned OFF> DEV void lds_arrive_nowait() {
  asm volatile("s_mov_b64 exec, 1\n\tds_add_u32 %0, %1 offset:%2\n\ts_mov_b64 exec, -1" ::"v"(0u), "v"(1u), "i"(OFF) : "memory");
}
template <unsigned OFF, int N> DEV void lds_arrive_cnt() {
  asm volatile("s_waitcnt lgkmcnt(%3)\n\ts_mov_b64 exec, 1\n\tds_add_u32 %0, %1 offset:%2\n\ts_mov_b64 exec, -1" ::"v"(0u), "v"(1u), "i"(OFF), "i"(N) : "memory");
}
template <unsigned OFF> DEV void lds_arrive() {
  asm volatile("s_waitcnt lgkmcnt(0)\n\ts_mov_b64 exec, 1\n\tds_add_u32 %0, %1 offset:%2\n\ts_mov_b64 exec, -1" ::"v"(0u), "v"(1u), "i"(OFF) : "memory");
}
template <unsigned OFF> DEV unsigned lds_ticket() {
  unsigned old;
  asm volatile("s_waitcnt lgkmcnt(0)\n\ts_mov_b64 exec, 1\n\tds_add_rtn_u32 %0, %1, %2 offset:%3\n\ts_mov_b64 exec, -1\n\ts_waitcnt lgkmcnt(0)"
               : "=&v"(old) : "v"(0u), "v"(1u), "i"(OFF) : "memory");
  return rfl(old);
}
DEV void sched_fence() { __builtin_amdgcn_sched_barrier(0); }
DEV void poll_tags(const char* p, bool on, u64 tag) {
#pragma unroll 1
  for (unsigned n = 0; n < SPIN_CAP && __builtin_amdgcn_ballot_w64(on) != 0; ++n) {
    if (n) __builtin_amdgcn_s_sleep(2);
    if (on) on = gld64(p) != tag;
  }
  asm volatile("" ::: "memory");
}

#define STAMP(i) do { } while (0)
#define STAMP_ARGS
#define STAMP_PASS
struct Epoch { unsigned lo, hi; };
DEV u64 make_tag(const Epoch& ep) {
  unsigned lo, hi;
  asm volatile("s_nop 0\n\tv_readfirstlane_b32 %0, %2\n\tv_readfirstlane_b32 %1, %3\n\ts_nop 4" : "=s"(lo), "=s"(hi) : "v"(ep.lo), "v"(ep.hi) : "memory");
  return (((u64)hi << 32) | lo) + 0x9E3779B97F4A7C15ull;
}
struct c32 { float re, im; };
DEV c32 cmul(c32 a, c32 b) { return c32{a.re * b.re - a.im * b.im, a.re * b.im + a.im * b.re}; }
DEV void cmul4(c32 (&a)[4], c32 b) {
  float p[4], q[4];
#pragma unroll
  for (int i = 0; i < 4; ++i) p[i] = a[i].im * b.im;
#pragma unroll
  for (int i = 0; i < 4; ++i) q[i] = a[i].im * b.re;
  __builtin_amdgcn_sched_barrier(0);
#pragma unroll
  for (int i = 0; i < 4; ++i) p[i] = __builtin_fmaf(a[i].re, b.re, -p[i]);
#pragma unroll
  for (int i = 0; i < 4; ++i) q[i] = __builtin_fmaf(a[i].re, b.im, q[i]);
  __builtin_amdgcn_sched_barrier(0);
#pragma unroll
  for (int i = 0; i < 4; ++i) a[i] = c32{p[i], q[i]};
}
DEV c32 cpow(float e2, float rev, float p) {
  float m = __builtin_amdgcn_exp2f(e2 * p), a = __builtin_amdgcn_fractf(rev * p);
  return c32{m * __builtin_amdgcn_cosf(a), m * __builtin_amdgcn_sinf(a)};
}
constexpr u64 VMASK = 0x00C3FFFF00C3FFFFull;
DEV unsigned vaddr(unsigned lane) {
  const unsigned r = lane & 31;
  const unsigned sl = r < 4 ? r : r < 12 ? r + 6 : r < 16 ? r - 8 : r < 18 ? r + 2 : r - 14;
  return ((VMASK >> lane) & 1) ? (lane >> 5) * 320 + sl * 16 : (r == 24 || r == 28) ? 656 : 640;
}

DEV void ssm_kernel_gen(const Params& P, lptr sm, unsigned base, int d, int lane, const Epoch& ep, u64& tag STAMP_ARGS) {
  unsigned la[2], ai[2], cr[2], ci[2];
#pragma unroll
  for (int q = 0; q < 2; ++q) {
    int o = d * 128 + q * 64 + lane;
    la[q] = P.logA[o]; ai[q] = P.Aim[o]; cr[q] = P.Cre[o]; ci[q] = P.Cim[o];
  }
  const unsigned ldt = P.logdt[d], dsk = P.Dsk[d];
  const unsigned r = lane & 31, h = lane >> 5, q4 = (lane & 15) >> 2;
  const unsigned tw = opaque(base + lane * 272);
  const unsigned tr = opaque(base + (4 * h + (q4 >> 1)) * 272 + (q4 & 1) * 64 + ((lane >> 4) & 1) * 32 + (lane & 3) * 8);
  const unsigned c0a = opaque(base + (4 + r + 32 * h) * 16), c0b = opaque(base + (68 + r + 64 * h) * 16), c0c = opaque(base + (228 + r - 32 * h) * 16);
  const unsigned c0z = opaque(base + (lane < 16 ? (lane >> 2) * KC_STRIDE + (lane & 3) * 16 : 4160)), ca = opaque(base + 64 + lane * 16);
  asm volatile("s_barrier" ::: "memory");
  const float dt = rbf(__builtin_amdgcn_exp2f(bf2f(ldt) * LOG2E));
  float scale = fmaxf(fmaxf(fabsf(bf2f(cr[0])), fabsf(bf2f(ci[0]))), fmaxf(fabsf(bf2f(cr[1])), fabsf(bf2f(ci[1]))));
  scale = wave_max(scale);
  if (!(scale > 0.f)) scale = 1.f;
  asm volatile("" : "+v"(scale));
  tag = make_tag(ep);
  STAMP(29);
  const float inv = __builtin_amdgcn_rcpf(scale);
  f32x16 acc = zero16();
  u32x4 fa[8], fb[8];
#pragma unroll
  for (int q = 0; q < 2; ++q) {
    const float are = -rbf(__builtin_amdgcn_exp2f(bf2f(la[q]) * LOG2E)) * dt;
    const float e2 = are * LOG2E, rev = (bf2f(ai[q]) * dt) * 0.15915494309189535f;
    const c32 z1 = cpow(e2, rev, 1.f), z8 = cpow(e2, rev, 8.f), z64 = cpow(e2, rev, 64.f), y = cpow(e2, rev, 256.f);
    const c32 z2 = cmul(z1, z1), z16 = cmul(z8, z8), y2 = cmul(y, y), z128 = cmul(z64, z64);
    const c32 z4 = cmul(z2, z2), z32 = cmul(z16, z16), y4 = cmul(y2, y2), z192 = cmul(z128, z64);
    c32 e[4] = {c32{1.f, 0.f}, z8, z16, cmul(z16, z8)};
    f16x2 br[4], bi[4];
    br[0] = pk2h(e[0].re, e[1].re); bi[0] = pk2h(e[0].im, e[1].im); br[1] = pk2h(e[2].re, e[3].re); bi[1] = pk2h(e[2].im, e[3].im);
    if (q == 1) acc = mfma32h(fa[0], fb[0], acc);
    cmul4(e, z32);
    br[2] = pk2h(e[0].re, e[1].re); bi[2] = pk2h(e[0].im, e[1].im); br[3] = pk2h(e[2].re, e[3].re); bi[3] = pk2h(e[2].im, e[3].im);
    *(LAS u32x4*)(sm + tw) = cat4(br);
    if (q == 1) acc = mfma32h(fa[1], fb[1], acc);
    sched_fence();
#pragma unroll
    for (int a = 1; a < 4; ++a) {
      const c32 f = a == 1 ? z64 : a == 2 ? z128 : z192;
      const f16x2 fr = pk2h(f.re, f.re), fi = pk2h(f.im, f.im);
      f16x2 pr[4], pi[4];
#pragma unroll
      for (int k = 0; k < 4; ++k) pr[k] = __builtin_elementwise_fma(br[k], fr, -(bi[k] * fi));
      if (a == 1) *(LAS u32x4*)(sm + tw + 64) = cat4(bi);
      if (q == 1) acc = mfma32h(fa[2 * a], fb[2 * a], acc);
      *(LAS u32x4*)(sm + tw + a * 16) = cat4(pr);
      sched_fence();
#pragma unroll
      for (int k = 0; k < 4; ++k) pi[k] = __builtin_elementwise_fma(br[k], fi, bi[k] * fr);
      if (q == 1) acc = mfma32h(fa[2 * a + 1], fb[2 * a + 1], acc);
      *(LAS u32x4*)(sm + tw + 64 + a * 16) = cat4(pi);
      sched_fence();
    }
    const c32 c0 = {bf2f(cr[q]) * inv, -bf2f(ci[q]) * inv};
    const c32 w1 = {z1.re, -z1.im}, w2 = {z2.re, -z2.im}, w4 = {z4.re, -z4.im}, w8 = {z8.re, -z8.im};
    const c32 wy = {y.re, -y.im}, wy2 = {y2.re, -y2.im}, wy4 = {y4.re, -y4.im};
    c32 L[32];
    auto put = [&](int jb, bool im) __attribute__((always_inline)) {
      u32x4 w;
#pragma unroll
      for (int k = 0; k < 4; ++k) {
        const int a = 8 * jb + 2 * k;
        if (a >= 28) w[k] = 0;
        else if (a == 22) w[k] = pk_h(im ? L[a].im : L[a].re, 0.f);
        else w[k] = im ? pk_h(L[a].im, L[a + 1].im) : pk_h(L[a].re, L[a + 1].re);
      }
      *(LAS u32x4*)(sm + tw + 128 + (im ? 64 : 0) + jb * 16) = w;
      sched_fence();
    };
    L[0] = c0; L[1] = cmul(c0, w1); L[4] = cmul(c0, wy); L[16] = cmul(c0, wy2);
    L[19] = cmul(L[4], wy2); L[6] = cmul(L[4], w4);
    sched_fence();
    { c32 t[4] = {L[0], L[1], L[4], L[6]}; cmul4(t, w2); L[2] = t[0]; L[3] = t[1]; L[5] = t[2]; L[7] = t[3]; }
    put(0, false);
    { c32 t[4] = {L[0], L[4], L[16], L[19]}; cmul4(t, wy4); L[13] = t[0]; L[15] = t[1]; L[21] = t[2]; L[26] = t[3]; }
    put(0, true);
    { c32 t[4] = {L[0], L[1], L[2], L[3]}; cmul4(t, w4); L[8] = t[0]; L[9] = t[1]; L[10] = t[2]; L[11] = t[3]; }
    { c32 t[4] = {L[4], L[13], L[16], L[15]}; cmul4(t, w8); L[12] = t[0]; L[14] = t[1]; L[18] = t[2]; L[20] = t[3]; }
    put(1, false);
    { c32 t[4] = {L[21], L[19], L[26], L[26]}; cmul4(t, w8); L[22] = t[0]; L[25] = t[1]; L[27] = t[2]; }
    put(1, true);
    L[17] = cmul(L[16], w4); L[24] = cmul(L[19], w4);
    put(2, false); put(2, true); put(3, false); put(3, true);
    asm volatile("" ::: "memory");
    if (q == 0) STAMP(30);
#pragma unroll
    for (int s = 0; s < 8; ++s) {
      fa[s] = cat(lds_tr(sm, tr + s * 2176 + 128), lds_tr(sm, tr + s * 2176 + 544 + 128));
      fb[s] = cat(lds_tr(sm, tr + s * 2176), lds_tr(sm, tr + s * 2176 + 544));
    }
    asm volatile("" ::: "memory");
    sched_fence();
  }
#pragma unroll
  for (int s = 0; s < 8; ++s) acc = mfma32h(fa[s], fb[s], acc);
  STAMP(31);
  const float ks = 2.f * dt * __builtin_amdgcn_rcpf(inv);
  float v[16];
#pragma unroll
  for (int i = 0; i < 16; ++i) v[i] = acc[i] * ks;
  v[0] += lane == 0 ? bf2f(dsk) : 0.f;
  const bool hi = h, odd = r & 1;
  *(LAS u32x4*)(sm + c0a) = u32x4{pk_bf(v[0], hi ? v[0] : v[1]), pk_bf(hi ? v[1] : v[2], hi ? v[2] : v[3]), pk_bf(hi ? v[2] : v[4], hi ? v[2] : v[5]),
                                  pk_bf(hi ? v[3] : v[6], hi ? v[4] : v[7])};
#pragma unroll
  for (int w = 0; w < 2; ++w) {
    const float lo = v[5 + 2 * w], up = v[6 + 2 * w], a = v[8 + 3 * w], b = v[9 + 3 * w], c = v[10 + 3 * w];
    const float e0 = hi ? lo : a, e3 = hi ? up : c;
    *(LAS u32x4*)(sm + c0b + w * 512) = u32x4{pk_bf(e0, e0), pk_bf(e0, hi ? lo : b), pk_bf(hi ? (odd ? up : lo) : b, hi ? up : b), pk_bf(e3, e3)};
  }
  {
    const float lo = hi ? v[9] : v[14], up = hi ? v[10] : v[15];
    const unsigned ll = pk_bf(lo, lo);
    *(LAS u32x4*)(sm + c0c) = u32x4{ll, ll, pk_bf(odd ? up : lo, up), pk_bf(up, up)};
  }
  {
    const unsigned k01 = __builtin_amdgcn_readlane(pk_bf(v[0], v[1]), 0), k12 = __builtin_amdgcn_readlane(pk_bf(v[1], v[2]), 0);
    const unsigned z2 = lane == 15 ? k01 << 16 : 0u, z3 = lane == 7 ? k01 << 16 : lane == 11 ? k01 : lane == 15 ? k12 : 0u;
    if (lane < 17) *(LAS u32x4*)(sm + c0z) = u32x4{0, 0, z2, z3};
  }
  asm volatile("" ::: "memory");
  u32x4 w[4];
  u32x2 n[4];
#pragma unroll
  for (int it = 0; it < 4; ++it) { w[it] = lds128(sm, ca + it * 1024); n[it] = lds64(sm, ca + it * 1024 + 16); }
  sched_fence();
#pragma unroll
  for (int it = 0; it < 4; ++it) {
    const unsigned a0 = __builtin_amdgcn_alignbit(w[it].y, w[it].x, 16), a1 = __builtin_amdgcn_alignbit(w[it].z, w[it].y, 16),
                   a2 = __builtin_amdgcn_alignbit(w[it].w, w[it].z, 16), a3 = __builtin_amdgcn_alignbit(n[it].x, w[it].w, 16),
                   a4 = __builtin_amdgcn_alignbit(n[it].y, n[it].x, 16);
    *(LAS u32x4*)(sm + ca + it * 1024 + 2 * KC_STRIDE) = u32x4{w[it].y, w[it].z, w[it].w, n[it].x};
    *(LAS u32x4*)(sm + ca + it * 1024 + KC_STRIDE) = u32x4{a0, a1, a2, a3};
    *(LAS u32x4*)(sm + ca + it * 1024 + 3 * KC_STRIDE) = u32x4{a1, a2, a3, a4};
  }
}

DEV void conv_step(lptr sm, unsigned fa, unsigned ub, f32x16& y0, f32x16& y1) {
#pragma unroll
  for (int s = 0; s < 2; ++s) {
    u32x4 fn = cat(lds64(sm, fa + 32 - 32 * s), lds64(sm, fa + 40 - 32 * s));
    u32x4 ff = cat(lds64(sm, fa + 2080 - 32 * s), lds64(sm, fa + 2088 - 32 * s));
    u32x4 b0 = lds128(sm, ub + 128 * s), b1 = lds128(sm, ub + 32 * UCOL + 128 * s);
    y1 = mfma32(fn, b1, y1);
    y0 = mfma32(fn, b0, y0);
    y1 = mfma32(ff, b0, y1);
  }
}

DEV void kv_project(const Params& P, lptr sm, int kt, int sw, int lane, const Epoch& ep, u64& tag, __amdgpu_buffer_rsrc_t wsr STAMP_ARGS) {
  const unsigned base = L_KC + sw * KC_W;
  const unsigned LW = base + 10240;
  const char* xb = (const char*)P.x + (size_t)kt * 32 * ROWB + sw * 320;
#pragma unroll
  for (int q = 0; q < 5; ++q) {
    const unsigned i = q * 64 + lane, row = (i * 3277) >> 16, pos = i - row * 20, rot = row >> 2;
    const unsigned vo = row * ROWB + (pos >= rot ? pos - rot : pos + 20 - rot) * 16;
    dma16<0, false>(xb, vo, base + q * 1024);
    dma16<0, false>(xb + 16 * ROWB, vo, base + 5120 + q * 1024);
  }
  const char* kb = (const char*)P.wk + sw * 6400;
#pragma unroll
  for (int q = 0; q < 7; ++q)
    if (q < 6 || lane < 16) dma16<0, false>(kb, q * 1024 + lane * 16, LW + q * 1024);
  const unsigned LV = L_QST + 3 * QSLOT + sw * 6400;
  const char* vb = (const char*)P.wv + sw * 6400;
#pragma unroll
  for (int q = 0; q < 7; ++q)
    if (q < 6 || lane < 16) dma16<0, false>(vb, q * 1024 + lane * 16, LV + q * 1024);
  asm volatile("s_barrier" ::: "memory");
  lds_arrive_nowait<M_KVISS>();
  const unsigned r = lane & 31, h = lane >> 5, g1 = (lane >> 4) & 1, q4 = (lane & 15) >> 2, p4 = lane & 3;
  const unsigned wa = opaque(LW + (8 * h + q4) * 40 + (g1 ? 32 : p4 * 8));
  const unsigned xk0 = opaque(base + r * 320 + (h + ((r >> 2) & 3)) * 16);
  const unsigned tm = (kt + 63) & 63, co = WS_KV + (tm >> 3) * CHUNK;
  const bool act = (VMASK >> lane) & 1;
  const unsigned so = opaque(sw == 0   ? co + (tm & 7) * REC + lane * 16
                             : sw == 1 ? co + QUAD_OFF + ((tm >> 2) & 1) * 1024 + lane * 16 + (tm & 3) * 4
                             : (act || lane == 24 || lane == 25) ? WS_KV + (kt >> 3) * CHUNK + (kt & 7) * REC + 1024 + (sw - 2) * V1_OFF + vaddr(lane)
                                                                 : 0x40000000u);
  const unsigned keep = opaque((sw < 2 || act) ? 0xffffffffu : 0u), cst = opaque((sw >= 2 && lane == 24) ? 0x3f803f80u : 0u);
  unsigned pw[3];
#pragma unroll
  for (int w = 0; w < 3; ++w) pw[w] = opaque(L_KC + (sw == 0 ? 0 : sw == 1 ? 2 : 2 * sw - 1) * 1024 + lane * 16 + ((sw + 1 + w) & 3) * KC_W);
  STAMP(2);
  wait_vm<0>();
  STAMP(3);
  tag = make_tag(ep);
  const unsigned va = opaque(LV + (8 * h + q4) * 40 + (g1 ? 32 : p4 * 8));
  f32x16 aK = zero16(), aV = zero16();
  u32x4 xk[10], fk[10], fv[10];
#pragma unroll
  for (int s = 0; s < 10; ++s) {
    xk[s] = lds128(sm, xk0 + s * 32 - ((s >= 8 && 2 * s + h + ((r >> 2) & 3) >= 20) ? 320 : 0));
    fv[s] = cat(lds_tr(sm, va + s * 640), lds_tr(sm, va + s * 640 + 160));
    fk[s] = cat(lds_tr(sm, wa + s * 640), lds_tr(sm, wa + s * 640 + 160));
    if (s >= 2) {
      sched_fence();
      aV = mfma32(xk[s - 2], fv[s - 2], aV);
      aK = mfma32(fk[s - 2], xk[s - 2], aK);
      sched_fence();
    }
  }
  lds_arrive_nowait<M_KVRD>();
#pragma unroll
  for (int s = 8; s < 10; ++s) { aV = mfma32(xk[s], fv[s], aV); aK = mfma32(fk[s], xk[s], aK); }
  const f32x4 part[7] = {f32x4{aK[0], aK[1], aK[2], aK[3]}, f32x4{aK[4], aK[5], aK[6], aK[7]}, f32x4{aK[8], aK[9], aK[10], aK[11]},
                         f32x4{aV[0], aV[1], aV[2], aV[3]}, f32x4{aV[4], aV[5], aV[6], aV[7]}, f32x4{aV[8], aV[9], aV[10], aV[11]},
                         f32x4{aV[12], aV[13], aV[14], aV[15]}};
  asm volatile("" ::: "memory");
#pragma unroll
  for (int i = 6; i >= 0; --i)
    if (sw != (i < 2 ? 0 : i == 2 ? 1 : i < 5 ? 2 : 3)) *(LAS f32x4*)(sm + base + i * 1024 + lane * 16) = part[i];
  f32x4 t0 = sw == 0 ? part[0] : sw == 1 ? part[2] : sw == 2 ? part[3] : part[5];
  f32x4 t1 = sw == 0 ? part[1] : sw == 1 ? part[2] : sw == 2 ? part[4] : part[6];
  asm volatile("" : "+v"(t0), "+v"(t1));
  STAMP(29);
  lds_arrive<M_CNT_S>();
  lds_wait_ge<false>(sm, M_CNT_S, 4);
  STAMP(30);
  f32x4 p0[3], p1[3];
#pragma unroll
  for (int w = 0; w < 3; ++w) { p0[w] = *(LAS f32x4*)(sm + pw[w]); p1[w] = *(LAS f32x4*)(sm + pw[w] + 1024); }
  sched_fence();
  t0 = (t0 + p0[0]) + (p0[1] + p0[2]); t1 = (t1 + p1[0]) + (p1[1] + p1[2]);
  const float ksc = sw < 2 ? 0.22360679774997896f * LOG2E : 1.f;
  t0 *= ksc; t1 *= ksc;
  u32x4 pk = {pk_bf(t0[0], t0[1]), pk_bf(t0[2], t0[3]), pk_bf(t1[0], t1[1]), pk_bf(t1[2], t1[3])};
  {
    auto s0 = __builtin_amdgcn_permlane32_swap(pk.x, pk.z, false, false);
    auto s1 = __builtin_amdgcn_permlane32_swap(pk.y, pk.w, false, false);
    const unsigned a0 = s0[0], a2 = s0[1], a1 = s1[0], a3 = s1[1];
    const bool v = sw >= 2, k8 = sw == 1;
    pk = u32x4{v ? a0 : (k8 && h) ? pk.y : pk.x, v ? a1 : k8 ? 0u : pk.y, v ? a2 : k8 ? 0u : pk.z, v ? a3 : k8 ? 0u : pk.w};
  }
  const u32x4 pm = pk & keep;
  if (sw == 1) __builtin_amdgcn_raw_buffer_store_b32(pk.x, wsr, so, 0, 16);
  else __builtin_amdgcn_raw_buffer_store_b128(pm | cst, wsr, so, 0, 16);
  float st = dot2bf(pm.x, pm.x, dot2bf(pm.y, pm.y, dot2bf(pm.z, pm.z, dot2bf(pm.w, pm.w, 0.f))));
  st += xhalf(st);
  if (!(st <= 1.0e38f)) st = 1.0e38f;
  st = wave_max(st);
  lds_st(sm, M_ST + sw * 4, __builtin_bit_cast(unsigned, st));
  STAMP(5);
  wait_vm<0>();
  STAMP(6);
  if (lds_ticket<M_CNT_S2>() == 3) {
    const u32x4 sv = lds128(sm, M_ST);
    const unsigned s0 = sv.x, s1 = sv.y, s2 = sv.z, s3 = sv.w;
    const float km = __builtin_bit_cast(float, s0) + __builtin_bit_cast(float, s1);
    if (lane < 8)
      __builtin_amdgcn_raw_buffer_store_b128(u32x4{__builtin_bit_cast(unsigned, km), (unsigned)tag, max(s2, s3), (unsigned)(tag >> 32)}, wsr,
                                             WS_FKV + (lane * 64 + kt) * FL, 0, 16);
  }
}

DEV unsigned o_lane(unsigned l) { return ((l >> 4) & 1) * 20480 + ((l >> 5) & 1) * 256 + (l & 15) * 16; }
DEV unsigned o_main(int c, int j, int w) { return WS_OF + (80 * c + (j >> 2) * 10 + ((2 * j + (w >> 1)) & 7)) * 1024 + (w & 1) * 512; }
DEV unsigned o_left(int c, int j) { return WS_OF + (80 * c + (j >> 2) * 10 + 8 + ((j >> 1) & 1)) * 1024 + (j & 1) * 512; }

DEV void fetch_a(const Params& P, int c, int j, int w, unsigned lane) {
  const char* ab = P.ws + WS_AT + (j * 80) * 4096 + c * 128;
#pragma unroll
  for (int k = 0; k < 3; ++k) {
    const int q = w + 4 * k;
    if (q < 10) dma16<0, true>(ab + q * 8 * 4096, (lane >> 3) * 4096 + (lane & 7) * 16, L_ATILE + q * 1024);
  }
}
DEV void dma_chunk(const Params& P, int c, int sw, int lane) {
  const char* src = P.ws + WS_KV + c * CHUNK + sw * 5120 + 2048;
  const unsigned dst = L_RING + (c & 1) * CHUNK + sw * 5120 + 2048;
  dma16<-2048, true>(src, lane * 16, dst); dma16<-1024, true>(src, lane * 16, dst); dma16<0, true>(src, lane * 16, dst);
  dma16<1024, true>(src, lane * 16, dst); dma16<2048, true>(src, lane * 16, dst);
  if (sw == 3 && lane < 32) dma16<3072, true>(src, lane * 16, dst);
}

DEV void attn_tile_guarded(u32x4 k16, unsigned k8, u32x4 q16, unsigned q8, u32x4 v0, u32x4 v1, float& m, f32x16& o) {
  f32x16 s = mfma32(u32x4{k8, 0, 0, 0}, u32x4{q8, 0, 0, 0}, zero16());
  s = mfma32(k16, q16, s);
  float t = s[0];
#pragma unroll
  for (int i = 1; i < 16; ++i) t = fmaxf(t, s[i]);
  t = fmaxf(t, xhalf(t));
  const float mn = fmaxf(m, t);
  const float al = __builtin_amdgcn_exp2f(m - mn);
  m = mn;
  f32x8 p0, p1;
#pragma unroll
  for (int i = 0; i < 8; ++i) { p0[i] = __builtin_amdgcn_exp2f(s[i] - mn); p1[i] = __builtin_amdgcn_exp2f(s[8 + i] - mn); }
  o *= al;
  o = __builtin_amdgcn_smfmac_f32_32x32x32_bf16(__builtin_bit_cast(bf16x8, v0), __builtin_bit_cast(bf16x16, p0), o, (int)0xDDDDDDDDu, 0, 0);
  o = __builtin_amdgcn_smfmac_f32_32x32x32_bf16(__builtin_bit_cast(bf16x8, v1), __builtin_bit_cast(bf16x16, p1), o, (int)0xDDDDDDDDu, 0, 0);
}

#if defined(__HIP_DEVICE_COMPILE__)
#define NO_PK __attribute__((target("no-packed-fp32-ops")))
#else
#define NO_PK
#endif
__global__ void __launch_bounds__(512, 2) NO_PK fused_kernel(Params P) {
  extern __shared__ __attribute__((aligned(16))) char smem_raw[];
  lptr sm = (lptr)smem_raw;
  const int tid = threadIdx.x, lane = tid & 63;
  {
    const char* pc;
    asm volatile("s_getpc_b64 %0" : "=s"(pc));
    const unsigned o = ((blockIdx.x >> 3) * PFW + tid) * 128;
    if (tid < PFW && o < CODE_AFTER) touch(pc, o);
  }
  const int wave = rfl(tid >> 6);
  const int bx = blockIdx.x & 7, bi = blockIdx.x >> 3;
  const int j = (int)(((bi & 16) ? J_HI : J_LO) >> (4 * (bi & 15))) & 7;
  const int cl = (int)(CL_TAB >> (2 * bi)) & 3;
  const int c = bx * 4 + cl;
  const bool ssm = j < 5;
  const unsigned r = lane & 31, h = lane >> 5, g = lane >> 4, q4 = (lane & 15) >> 2, p4 = lane & 3;
  const __amdgpu_buffer_rsrc_t wsr = __builtin_amdgcn_make_buffer_rsrc((void*)P.ws, 0, WS_LAST, 0x00020000);

  STAMP(0);
  u64 tag;
  bool a_todo = true;
  Epoch ep;
  asm volatile("global_load_dword %0, %2, %3 sc1\n\tglobal_load_dword %1, %2, %3 offset:4 sc1" : "=&v"(ep.lo), "=&v"(ep.hi) : "v"(0u), "s"(P.ws) : "memory");
  if (wave < 4) {
#define APRIO 0
    __builtin_amdgcn_s_setprio(APRIO);
    const int kc0 = (j * 160) >> 7;
    auto kchunk = [&](int i) __attribute__((always_inline)) {
      const int k = kc0 + (i < 2 ? i : 2 + ((i - 2 + 2 * cl) & 7));
      return k >= 10 ? k - 10 : k;
    };
    const unsigned sg = (r + 1) & 3;
    unsigned ak, av, fa, ub, q16o, q8o, uo6[6];
    auto att_addr = [&]() __attribute__((always_inline)) {
      ak = opaque(L_FIRST + lane * 16);
      av = opaque(L_RING + 1024 + vaddr(lane));
      fa = opaque(L_KC + wave * KC_W + sg * KC_STRIDE + 2 * (r - 8 * h - 7 - sg + 32) - 32);
      ub = opaque(L_UP + (31 + r) * UCOL + h * 64 + wave * 16);
      q16o = opaque(L_QX + wave * 2048 + lane * 16);
      q8o = opaque(L_QX + 8192 + ((wave & 1) * 32 + r) * 16 + (2 * (wave >> 1) + h) * 4);
    };
    if (wave == 3) {
      const __amdgpu_buffer_rsrc_t xr = __builtin_amdgcn_make_buffer_rsrc((void*)P.x, 0, SEQ * ROWB, 0x00020000);
      const __amdgpu_buffer_rsrc_t wr = __builtin_amdgcn_make_buffer_rsrc((void*)P.wq, 0, DM * ROWB, 0x00020000);
      lds_st(sm, lane * 4, (lane >> 4) == 1);
      unsigned xo[4], wo_[5];
#pragma unroll
      for (int q = 0; q < 4; ++q) {
        const unsigned row = q * 8 + (lane >> 3);
        xo[q] = (c * 64 + row) * ROWB + (((lane & 7) ^ ((row >> 1) & 7)) * 16) - q * 1024;
      }
#pragma unroll
      for (int q = 0; q < 5; ++q) {
        const unsigned i = q * 64 + lane;
        wo_[q] = ((i * 6554) >> 16) * (ROWB - 160) + i * 16 + j * 160 - (q < 3 ? q : q - 3) * 1024;
      }
      wait_lgkm0();
      if (j == 5 || j == 6) { hw_barrier(); lds_wait_ge<true>(sm, M_KVISS, 4); }
      const unsigned oob = opaque(0x40000000u);
      unsigned pr = 0, l16;
#pragma unroll
      for (int i = 0; i < 10; ++i) {
        if (i == 7) STAMP(19);
        if (i >= 5) {
          if (__builtin_expect(rfl(pr) < i - 4, 0)) wait_prog<false>(sm, i - 4);
          asm volatile("" ::: "memory");
        }
        if (i == 3 && (j == 5 || j == 6)) lds_wait_ge<false>(sm, M_KVRD, 4);
        if (i == 0) STAMP(12);
        if (i == 5) STAMP(11);
        if (i == 7) STAMP(20);
        if (i == 8) STAMP(21);
        if (i >= 2) wait_vm<42>();
        if (i == 8) tag = make_tag(ep);
        const int kc = kchunk(i);
        dma_slot(xr, wr, xo, wo_, kc * 128, kc * 64 * ROWB, L_QST + (i % 5) * QSLOT);
        if (i == 0) {
          dma16<0, false>(P.gw, lane * 16, L_GW);
          if (lane < 16) dma16<1024, false>(P.gw, lane * 16, L_GW);
        }
        if (i >= 5 && i < 9) pr = prog_min(sm);
        dma_mark(wr, oob, M_RAW + 4 * i);
        if (i == 0 && j != 5 && j != 6) hw_barrier();
        if (i == 4) {
          l16 = opaque(lane * 16);
          att_addr();
        }
      }
      STAMP(2);
      u32x4 f = {0, ~(unsigned)tag, 0, 0};
      bool wk = true, wu = ssm && lane < 32, u_todo = true;
      const char* src = P.ws + WS_KV;
      wait_prog<false>(sm, POLL_AT);
#pragma unroll 1
      for (unsigned n = 0; n < SPIN_CAP; ++n) {
        asm volatile("" ::: "memory");
        u64 uv = 0;
        if (wk) f = __builtin_amdgcn_raw_buffer_load_b128(wsr, WS_FKV + (bx * 64 + lane) * FL, 0, 16);
        if (wu) uv = gld64(P.ws + WS_FUT + (bx * 32 + r) * FL);
        wk = f.y != (unsigned)tag || f.w != (unsigned)(tag >> 32);
        wu = wu && uv != tag;
        if (u_todo && __builtin_amdgcn_ballot_w64(wu) == 0) { u_todo = false; lds_st(sm, M_URDY, 1); }
        if (__builtin_amdgcn_ballot_w64(wk) == 0) break;
      }
      asm volatile("" ::: "memory");
      STAMP(4);
      wait_prog<false>(sm, 9);
      dma16<0, true>(src + 7 * CHUNK + 7 * REC, l16, L_FIRST);
      dma16<0, true>(src + 7 * CHUNK + QUAD_OFF + 1024, l16, L_FIRST + 1024);
      dma_run<true>(src, l16, L_RING); dma_run<true>(src + 16896, l16, L_RING + 16896);
      dma_run<true>(src + 4096, l16, L_RING + 4096); dma_run<true>(src + 8192, l16, L_RING + 8192);
      dma_run<true>(src + 12288, l16, L_RING + 12288);
      dma16<0, true>(src + 16384, l16, L_RING + 16384);
      STAMP(5);
      const unsigned fk = f.x, fv = f.z;
      const float kx = wave_max(__builtin_bit_cast(float, fk)), vx = wave_max(__builtin_bit_cast(float, fv));
      lds_st(sm, M_KMAX, __builtin_bit_cast(unsigned, (vx <= 1.0e24f && vx >= 1.0e-20f) ? kx : __builtin_inff()));
      lds_st(sm, M_KVRDY, 1);
      if (u_todo) {
        poll_tags(P.ws + WS_FUT + (bx * 32 + r) * FL, wu, tag);
        lds_st(sm, M_URDY, 1);
      }
      STAMP(6);
    } else {
      unsigned xa[2][4], wa[2];
#pragma unroll
      for (int hi = 0; hi < 2; ++hi) {
#pragma unroll
        for (int s = 0; s < 4; ++s) xa[hi][s] = opaque(L_QST + hi * 3 * QSLOT + r * 128 + (((2 * s + h) ^ ((r >> 1) & 7)) * 16));
        wa[hi] = opaque(L_QST + hi * 3 * QSLOT + 8192 + (8 * h + q4) * 160 + (wave == 2 ? p4 * 40 + 32 : wave * 80 + (g & 1) * 40 + p4 * 8));
      }
      const unsigned ga = opaque(L_GW + h * 16);
      unsigned ua[4][2], uo[4];
      u32x4 ut[4];
#pragma unroll
      for (int k = 0; k < 4; ++k) {
        const unsigned cq4 = 2 * min(wave + 3 * k, 9) + (g >> 1), oc = 4 * (g & 1) + p4;
        const unsigned by = j * 160 + cq4 * 8 - kc0 * 128;
        const unsigned r1 = 8 * oc + 7 - q4, r2 = r1 - 4;
        const unsigned cb = L_QST + (by >> 7) * QSLOT + (by & 8), c16 = (by >> 4) & 7;
        ua[k][0] = opaque(cb + r1 * 128 + ((c16 ^ ((r1 >> 1) & 7)) * 16));
        ua[k][1] = opaque(cb + r2 * 128 + ((c16 ^ ((r2 >> 1) & 7)) * 16));
        uo[k] = opaque(WS_UT + (j * 20 + cq4) * 16384 + c * 512 + (lane & 31) * 16);
      }
      f32x16 a0 = zero16(), a1 = zero16();
      att_addr();
#pragma unroll
      for (int k = 0; k < 6; ++k) {
        const unsigned i = (3 * k + wave) * 64 + lane, col = (i * 3856) >> 16;
        uo6[k] = opaque(col * 256 + min(i - col * 17, 15u) * 16);
      }
      const unsigned pa = opaque(M_PRG + wave * 4);
      unsigned cq = 0;
      hw_barrier();
      STAMP(1);
      auto qloop = [&](auto gate) __attribute__((always_inline)) {
        constexpr bool GATE = decltype(gate)::value;
        constexpr int NR = GATE ? 5 : 4;
        u32x4 xf[2][8], gq[2][4];
        u32x2 wl[2][4], wh[2][4];
        auto rd = [&](int i, int n) __attribute__((always_inline)) {
          const int hi = (i % 5) >= 3, s = n / NR, e = n % NR;
          const unsigned sl = ((i % 5) - 3 * hi) * QSLOT;
          if (e == 0) wl[i & 1][s] = lds_tr(sm, wa[hi] + sl + s * 2560);
          else if (e == 1) wh[i & 1][s] = lds_tr(sm, wa[hi] + sl + s * 2560 + 640);
          else if (e == NR - 2) xf[i & 1][2 * s] = lds128(sm, xa[hi][s] + sl);
          else if (e == NR - 1) xf[i & 1][2 * s + 1] = lds128(sm, xa[hi][s] + sl + 4096);
          else gq[i & 1][s] = lds128(sm, ga + kchunk(i) * 128 + s * 32);
        };
        lds_wait_zero(sm, M_RAW);
        STAMP(3);
#pragma unroll
        for (int n = 0; n < 4 * NR; ++n) rd(0, n);
#pragma unroll
        for (int i = 0; i < 10; ++i) {
          const int b = i & 1;
          unsigned cr = 0;
          u32x4 wfr;
          if (i == 1 || i == 9) STAMP(4 + i);
#pragma unroll
          for (int m = 0; m < 8; ++m) {
            sched_fence();
            if ((m & 1) == 0) {
              wfr = cat(wl[b][m >> 1], wh[b][m >> 1]);
              if (GATE) {
                const u32x4 gg = gq[b][m >> 1];
                wfr = u32x4{r >= 16 ? gg.x : wfr.x, r >= 16 ? gg.y : wfr.y, r >= 16 ? gg.z : wfr.z, r >= 16 ? gg.w : wfr.w};
              }
              a0 = mfma32(wfr, xf[b][m], a0);
            } else {
              a1 = mfma32(wfr, xf[b][m], a1);
            }
            sched_fence();
            if (i < 9) {
              if (m == 0) cr = lds_ld(sm, M_RAW + 4 * (i + 1));
              if (m == 1) {
                if (__builtin_expect(rfl(cr) != 0, 0)) lds_wait_zero(sm, M_RAW + 4 * (i + 1));
                asm volatile("" ::: "memory");
              }
              if (i == 0 && m >= 2 && m < 6) ut[m - 2] = cat(lds_tr(sm, ua[m - 2][0]), lds_tr(sm, ua[m - 2][1]));
              if (i == 1 && (m & 1) == 1 && (m < 7 || wave == 0)) __builtin_amdgcn_raw_buffer_store_b128(ut[m >> 1], wsr, uo[m >> 1], 0, 16);
              if (m >= 1) {
#pragma unroll
                for (int n = (m - 1) * 4 * NR / 7; n < m * 4 * NR / 7; ++n) rd(i + 1, n);
              }
            }
          }
          sched_fence();
          if (i == 3) {
            wait_vm<0>();
            tag = make_tag(ep);
            if (lds_ticket<M_CNT_U>() == 2 && lane == 0) gst64(P.ws + WS_FUT + (j * 32 + c) * FL, tag);
          }
          if (i == 8) cq = prog_min(sm);
          asm volatile("" ::: "memory");
          lds_st(sm, pa, i + 2);
        }
      };
      if (wave == 2) qloop(std::true_type{}); else qloop(std::false_type{});
      STAMP(14);
      if (__builtin_expect(rfl(cq) < 8, 0)) wait_prog<false>(sm, 8);
      asm volatile("" ::: "memory");
      auto pk8 = [&](const f32x16& a, int o) __attribute__((always_inline)) {
        return u32x4{pk_bf(a[o], a[o + 1]), pk_bf(a[o + 2], a[o + 3]), pk_bf(a[o + 4], a[o + 5]), pk_bf(a[o + 6], a[o + 7])};
      };
      const unsigned qx = L_QX + wave * 4096 + lane * 16;
      *(LAS u32x4*)(sm + qx) = pk8(a0, 0);
      *(LAS u32x4*)(sm + qx + 1024) = pk8(a1, 0);
      if (wave < 2) {
        *(LAS u32x4*)(sm + qx + 2048) = pk8(a0, 8);
        *(LAS u32x4*)(sm + qx + 3072) = pk8(a1, 8);
      } else {
        *(LAS float*)(sm + L_Z + lane * 4) = h ? a1[8] : a0[8];
      }
    }
    if (wave < 3) lds_arrive<M_CNT_Q>();
    if (ssm && wave < 3) {
      const char* ubp = P.ws + WS_UT + (bx * 20 + cl * 5 + j) * 16384;
      lds_wait_ge<false>(sm, M_URDY, 1);
#pragma unroll
      for (int k = 0; k < 6; ++k)
        if (k < 5 || wave < 2) dma16<0, true>(ubp, uo6[k], L_UP + 31 * UCOL + (3 * k + wave) * 1024);
#pragma unroll
      for (int q = 0; q < 3; ++q)
        if (q < 2 || wave < 2 || lane < 15) *(LAS u32x4*)(sm + L_UP + (3 * q + wave) * 1024 + lane * 16) = u32x4{0, 0, 0, 0};
    }
    f32x16 oa = zero16(), ob = zero16();
    u32x4 v00 = {0, 0, 0, 0}, v10 = v00, v01 = v00, v11 = v00;
    asm volatile("" : "+v"(oa), "+v"(ob), "+v"(v00), "+v"(v10), "+v"(v01), "+v"(v11));
    lds_wait_ge<false>(sm, M_CNT_Q, 3);
    const u32x4 qa16 = lds128(sm, q16o), qb16 = lds128(sm, q16o + 1024);
    const unsigned qa8 = *(LAS unsigned*)(sm + q8o), qb8 = *(LAS unsigned*)(sm + q8o + 1024);
    float na = dot2bf(qa8, qa8, 0.f), nb = dot2bf(qb8, qb8, 0.f);
#pragma unroll
    for (int i = 0; i < 4; ++i) {
      const unsigned ea = qa16[i], eb = qb16[i];
      na = dot2bf(ea, ea, na); nb = dot2bf(eb, eb, nb);
    }
    float qn;
    {
      auto t = __builtin_amdgcn_permlane32_swap(__builtin_bit_cast(unsigned, na), __builtin_bit_cast(unsigned, nb), false, false);
      const unsigned t0 = t[0], t1 = t[1];
      qn = __builtin_bit_cast(float, t0) + __builtin_bit_cast(float, t1);
    }
    lds_wait_ge<false>(sm, M_KVRDY, 1);
    const float km2 = __builtin_bit_cast(float, lds_ld(sm, M_KMAX));
    const bool safe = __builtin_amdgcn_ballot_w64(qn * km2 <= 3400.f) == ~0ull;
    if (safe) {
      u32x4 k0, k1;
      u32x2 qa8e = {qa8, 0}, qa8o = {0, qa8}, qb8e = {qb8, 0}, qb8o = {0, qb8};
      int ca = ssm ? 2 : 0, cb = ssm ? 1 : 8, dl = CHUNK;
      unsigned fb = fa + 2048, idx = 0xDDDDDDDDu;
      ub -= 17 * UCOL;
      asm volatile("" : "+v"(qa8e), "+v"(qa8o), "+v"(qb8e), "+v"(qb8o), "+v"(fb), "+v"(ub), "+v"(idx), "+s"(ca), "+s"(cb), "+s"(dl));
      __builtin_amdgcn_s_setprio(0);
      wait_vm<0>();
      STAMP(15);
      hw_barrier();
      STAMP(16);
      asm volatile(
          "s_cmp_eq_u32 %[ca], 0\n\t"
          "s_cbranch_scc1 5f\n\t"
          "ds_read_b128 %[k0], %[ak]\n\t"
          "ds_read_b128 v[180:183], %[ak] offset:1024\n\t"
          "ds_read_b128 v[196:199], %[ub] offset:13328\n\t"
          "ds_read2_b64 v[184:187], %[fa] offset0:4 offset1:5\n\t"
          "ds_read_b128 v[192:195], %[ub] offset:4624\n\t"
          "ds_read2_b64 v[188:191], %[fb] offset0:4 offset1:5\n\t"
          "ds_read_b128 v[212:215], %[ub] offset:13456\n\t"
          "ds_read2_b64 v[200:203], %[fa] offset0:0 offset1:1\n\t"
          "ds_read_b128 v[208:211], %[ub] offset:4752\n\t"
          "v_mov_b32 v160, 0\n\t"
          "v_mov_b32 v161, 0\n\t"
          "v_mov_b32 v162, 0\n\t"
          "v_mov_b32 v163, 0\n\t"
          "v_mov_b32 v164, 0\n\t"
          "v_mov_b32 v165, 0\n\t"
          "v_mov_b32 v166, 0\n\t"
          "v_mov_b32 v167, 0\n\t"
          "v_mov_b32 v168, 0\n\t"
          "v_mov_b32 v169, 0\n\t"
          "v_mov_b32 v170, 0\n\t"
          "v_mov_b32 v171, 0\n\t"
          "v_mov_b32 v172, 0\n\t"
          "v_mov_b32 v173, 0\n\t"
          "v_mov_b32 v174, 0\n\t"
          "v_mov_b32 v175, 0\n\t"
          "v_mov_b32 v216, 0\n\t"
          "v_mov_b32 v217, 0\n\t"
          "v_mov_b32 v218, 0\n\t"
          "v_mov_b32 v219, 0\n\t"
          "v_mov_b32 v220, 0\n\t"
          "v_mov_b32 v221, 0\n\t"
          "v_mov_b32 v222, 0\n\t"
          "v_mov_b32 v223, 0\n\t"
          "v_mov_b32 v224, 0\n\t"
          "v_mov_b32 v225, 0\n\t"
          "v_mov_b32 v226, 0\n\t"
          "v_mov_b32 v227, 0\n\t"
          "v_mov_b32 v228, 0\n\t"
          "v_mov_b32 v229, 0\n\t"
          "v_mov_b32 v230, 0\n\t"
          "v_mov_b32 v231, 0\n\t"
          "v_mov_b32 v232, 0\n\t"
          "v_mov_b32 v233, 0\n\t"
          "v_mov_b32 v234, 0\n\t"
          "v_mov_b32 v235, 0\n\t"
          "v_mov_b32 v236, 0\n\t"
          "v_mov_b32 v237, 0\n\t"
          "v_mov_b32 v238, 0\n\t"
          "v_mov_b32 v239, 0\n\t"
          "v_mov_b32 v240, 0\n\t"
          "v_mov_b32 v241, 0\n\t"
          "v_mov_b32 v242, 0\n\t"
          "v_mov_b32 v243, 0\n\t"
          "v_mov_b32 v244, 0\n\t"
          "v_mov_b32 v245, 0\n\t"
          "v_mov_b32 v246, 0\n\t"
          "v_mov_b32 v247, 0\n\t"
          "s_waitcnt lgkmcnt(0)\n\t"
          "v_mfma_f32_32x32x8_bf16 v[112:127], v[182:183], %[qa8o], 0\n\t"
          "v_mfma_f32_32x32x16_bf16 v[112:127], %[k0], %[qa16], v[112:127]\n\t"
          "v_mfma_f32_32x32x16_bf16 v[232:247], v[184:187], v[196:199], v[232:247]\n\t"
          "ds_read_b128 %[k1], %[ak] offset:2048\n\t"
          "ds_read_b128 v[176:179], %[ak] offset:20992\n\t"
          "v_mfma_f32_32x32x16_bf16 v[216:231], v[184:187], v[192:195], v[216:231]\n\t"
          "ds_read2_b64 v[204:207], %[fb] offset0:0 offset1:1\n\t"
          "ds_read_b128 %[v00], %[av] offset:0\n\t"
          "ds_read_b128 %[v10], %[av] offset:672\n\t"
          "s_nop 7\n\t"
          "s_nop 3\n\t"
          "s_branch 2f\n\t"
          "1:\n\t"
          "v_mfma_f32_32x32x8_bf16 v[112:127], v[182:183], %[qa8o], 0\n\t"
          "v_exp_f32 v160, v160\n\t"
          "v_exp_f32 v161, v161\n\t"
          "v_exp_f32 v162, v162\n\t"
          "v_exp_f32 v163, v163\n\t"
          "v_mfma_f32_32x32x16_bf16 v[112:127], %[k0], %[qa16], v[112:127]\n\t"
          "v_exp_f32 v164, v164\n\t"
          "v_exp_f32 v165, v165\n\t"
          "v_exp_f32 v166, v166\n\t"
          "v_mfma_f32_32x32x16_bf16 v[232:247], v[184:187], v[196:199], v[232:247]\n\t"
          "v_exp_f32 v167, v167\n\t"
          "v_exp_f32 v168, v168\n\t"
          "v_exp_f32 v169, v169\n\t"
          "v_smfmac_f32_32x32x32_bf16 %[oa], %[v01], v[144:151], %[idx]\n\t"
          "ds_read_b128 %[k1], %[ak] offset:2048\n\t"
          "ds_read_b128 v[176:179], %[ak] offset:20992\n\t"
          "v_exp_f32 v170, v170\n\t"
          "v_mfma_f32_32x32x16_bf16 v[216:231], v[184:187], v[192:195], v[216:231]\n\t"
          "v_exp_f32 v171, v171\n\t"
          "v_exp_f32 v172, v172\n\t"
          "v_exp_f32 v173, v173\n\t"
          "v_smfmac_f32_32x32x32_bf16 %[oa], %[v11], v[152:159], %[idx]\n\t"
          "ds_read2_b64 v[204:207], %[fb] offset0:0 offset1:1\n\t"
          "ds_read_b128 %[v00], %[av] offset:0\n\t"
          "ds_read_b128 %[v10], %[av] offset:672\n\t"
          "v_exp_f32 v174, v174\n\t"
          "v_exp_f32 v175, v175\n\t"
          "2:\n\t"
          "v_mfma_f32_32x32x8_bf16 v[128:143], v[182:183], %[qb8o], 0\n\t"
          "v_exp_f32 v112, v112\n\t"
          "v_exp_f32 v113, v113\n\t"
          "v_exp_f32 v114, v114\n\t"
          "v_exp_f32 v115, v115\n\t"
          "v_mfma_f32_32x32x16_bf16 v[128:143], %[k0], %[qb16], v[128:143]\n\t"
          "s_waitcnt lgkmcnt(7)\n\t"
          "v_exp_f32 v116, v116\n\t"
          "v_exp_f32 v117, v117\n\t"
          "v_exp_f32 v118, v118\n\t"
          "v_mfma_f32_32x32x16_bf16 v[232:247], v[188:191], v[192:195], v[232:247]\n\t"
          "v_exp_f32 v119, v119\n\t"
          "v_exp_f32 v120, v120\n\t"
          "v_exp_f32 v121, v121\n\t"
          "v_smfmac_f32_32x32x32_bf16 %[ob], %[v01], v[160:167], %[idx]\n\t"
          "s_waitcnt lgkmcnt(5)\n\t"
          "ds_read2_b64 v[184:187], %[fa] offset0:12 offset1:13\n\t"
          "ds_read_b128 v[196:199], %[ub] offset:13056\n\t"
          "v_exp_f32 v122, v122\n\t"
          "v_mfma_f32_32x32x16_bf16 v[232:247], v[200:203], v[212:215], v[232:247]\n\t"
          "v_exp_f32 v123, v123\n\t"
          "v_exp_f32 v124, v124\n\t"
          "v_exp_f32 v125, v125\n\t"
          "v_smfmac_f32_32x32x32_bf16 %[ob], %[v11], v[168:175], %[idx]\n\t"
          "s_waitcnt lgkmcnt(2)\n\t"
          "ds_read2_b64 v[188:191], %[fb] offset0:12 offset1:13\n\t"
          "ds_read_b128 v[192:195], %[ub] offset:4352\n\t"
          "v_exp_f32 v126, v126\n\t"
          "v_exp_f32 v127, v127\n\t"
          "v_mfma_f32_32x32x8_bf16 v[144:159], v[176:177], %[qa8e], 0\n\t"
          "v_exp_f32 v128, v128\n\t"
          "v_exp_f32 v129, v129\n\t"
          "v_exp_f32 v130, v130\n\t"
          "v_exp_f32 v131, v131\n\t"
          "v_mfma_f32_32x32x16_bf16 v[144:159], %[k1], %[qa16], v[144:159]\n\t"
          "v_exp_f32 v132, v132\n\t"
          "v_exp_f32 v133, v133\n\t"
          "v_exp_f32 v134, v134\n\t"
          "v_mfma_f32_32x32x16_bf16 v[216:231], v[200:203], v[208:211], v[216:231]\n\t"
          "v_exp_f32 v135, v135\n\t"
          "v_exp_f32 v136, v136\n\t"
          "v_exp_f32 v137, v137\n\t"
          "v_smfmac_f32_32x32x32_bf16 %[oa], %[v00], v[112:119], %[idx]\n\t"
          "ds_read_b128 v[212:215], %[ub] offset:13184\n\t"
          "ds_read_b128 %[k0], %[ak] offset:4416\n\t"
          "v_exp_f32 v138, v138\n\t"
          "v_mfma_f32_32x32x16_bf16 v[232:247], v[204:207], v[208:211], v[232:247]\n\t"
          "v_exp_f32 v139, v139\n\t"
          "v_exp_f32 v140, v140\n\t"
          "v_exp_f32 v141, v141\n\t"
          "v_smfmac_f32_32x32x32_bf16 %[oa], %[v10], v[120:127], %[idx]\n\t"
          "ds_read2_b64 v[200:203], %[fa] offset0:8 offset1:9\n\t"
          "ds_read_b128 %[v01], %[av] offset:2368\n\t"
          "ds_read_b128 %[v11], %[av] offset:3040\n\t"
          "v_exp_f32 v142, v142\n\t"
          "v_mfma_f32_32x32x8_bf16 v[160:175], v[176:177], %[qb8e], 0\n\t"
          "v_exp_f32 v143, v143\n\t"
          "v_exp_f32 v144, v144\n\t"
          "v_exp_f32 v145, v145\n\t"
          "v_exp_f32 v146, v146\n\t"
          "v_mfma_f32_32x32x16_bf16 v[160:175], %[k1], %[qb16], v[160:175]\n\t"
          "s_waitcnt lgkmcnt(7)\n\t"
          "v_exp_f32 v147, v147\n\t"
          "v_exp_f32 v148, v148\n\t"
          "v_exp_f32 v149, v149\n\t"
          "v_mfma_f32_32x32x16_bf16 v[232:247], v[184:187], v[196:199], v[232:247]\n\t"
          "v_exp_f32 v150, v150\n\t"
          "v_exp_f32 v151, v151\n\t"
          "v_exp_f32 v152, v152\n\t"
          "v_smfmac_f32_32x32x32_bf16 %[ob], %[v00], v[128:135], %[idx]\n\t"
          "s_waitcnt lgkmcnt(5)\n\t"
          "ds_read2_b64 v[204:207], %[fb] offset0:8 offset1:9\n\t"
          "ds_read_b128 v[208:211], %[ub] offset:4480\n\t"
          "v_exp_f32 v153, v153\n\t"
          "v_mfma_f32_32x32x16_bf16 v[216:231], v[184:187], v[192:195], v[216:231]\n\t"
          "v_exp_f32 v154, v154\n\t"
          "v_exp_f32 v155, v155\n\t"
          "v_exp_f32 v156, v156\n\t"
          "v_smfmac_f32_32x32x32_bf16 %[ob], %[v10], v[136:143], %[idx]\n\t"
          "s_waitcnt lgkmcnt(2)\n\t"
          "ds_read2_b64 v[184:187], %[fa] offset0:20 offset1:21\n\t"
          "ds_read_b128 v[196:199], %[ub] offset:12784\n\t"
          "v_exp_f32 v157, v157\n\t"
          "v_exp_f32 v158, v158\n\t"
          "v_mfma_f32_32x32x8_bf16 v[112:127], v[176:177], %[qa8o], 0\n\t"
          "v_exp_f32 v159, v159\n\t"
          "v_exp_f32 v160, v160\n\t"
          "v_exp_f32 v161, v161\n\t"
          "v_exp_f32 v162, v162\n\t"
          "v_mfma_f32_32x32x16_bf16 v[112:127], %[k0], %[qa16], v[112:127]\n\t"
          "v_exp_f32 v163, v163\n\t"
          "v_exp_f32 v164, v164\n\t"
          "v_exp_f32 v165, v165\n\t"
          "v_mfma_f32_32x32x16_bf16 v[232:247], v[188:191], v[192:195], v[232:247]\n\t"
          "v_exp_f32 v166, v166\n\t"
          "v_exp_f32 v167, v167\n\t"
          "v_exp_f32 v168, v168\n\t"
          "v_smfmac_f32_32x32x32_bf16 %[oa], %[v01], v[144:151], %[idx]\n\t"
          "ds_read_b128 v[192:195], %[ub] offset:4080\n\t"
          "ds_read_b128 %[k1], %[ak] offset:6784\n\t"
          "v_exp_f32 v169, v169\n\t"
          "v_mfma_f32_32x32x16_bf16 v[232:247], v[200:203], v[212:215], v[232:247]\n\t"
          "v_exp_f32 v170, v170\n\t"
          "v_exp_f32 v171, v171\n\t"
          "v_exp_f32 v172, v172\n\t"
          "v_smfmac_f32_32x32x32_bf16 %[oa], %[v11], v[152:159], %[idx]\n\t"
          "ds_read_b128 %[v00], %[av] offset:4736\n\t"
          "ds_read_b128 %[v10], %[av] offset:5408\n\t"
          "v_exp_f32 v173, v173\n\t"
          "v_exp_f32 v174, v174\n\t"
          "v_mfma_f32_32x32x8_bf16 v[128:143], v[176:177], %[qb8o], 0\n\t"
          "v_exp_f32 v175, v175\n\t"
          "v_exp_f32 v112, v112\n\t"
          "v_exp_f32 v113, v113\n\t"
          "v_exp_f32 v114, v114\n\t"
          "v_mfma_f32_32x32x16_bf16 v[128:143], %[k0], %[qb16], v[128:143]\n\t"
          "s_waitcnt lgkmcnt(6)\n\t"
          "v_exp_f32 v115, v115\n\t"
          "v_exp_f32 v116, v116\n\t"
          "v_exp_f32 v117, v117\n\t"
          "v_mfma_f32_32x32x16_bf16 v[216:231], v[200:203], v[208:211], v[216:231]\n\t"
          "v_exp_f32 v118, v118\n\t"
          "v_exp_f32 v119, v119\n\t"
          "v_exp_f32 v120, v120\n\t"
          "v_smfmac_f32_32x32x32_bf16 %[ob], %[v01], v[160:167], %[idx]\n\t"
          "ds_read_b128 v[212:215], %[ub] offset:12912\n\t"
          "ds_read2_b64 v[188:191], %[fb] offset0:20 offset1:21\n\t"
          "v_exp_f32 v121, v121\n\t"
          "v_mfma_f32_32x32x16_bf16 v[232:247], v[204:207], v[208:211], v[232:247]\n\t"
          "v_exp_f32 v122, v122\n\t"
          "v_exp_f32 v123, v123\n\t"
          "v_exp_f32 v124, v124\n\t"
          "v_smfmac_f32_32x32x32_bf16 %[ob], %[v11], v[168:175], %[idx]\n\t"
          "s_waitcnt lgkmcnt(2)\n\t"
          "ds_read_b128 v[208:211], %[ub] offset:4208\n\t"
          "ds_read2_b64 v[200:203], %[fa] offset0:16 offset1:17\n\t"
          "v_exp_f32 v125, v125\n\t"
          "v_exp_f32 v126, v126\n\t"
          "v_mfma_f32_32x32x8_bf16 v[144:159], v[178:179], %[qa8e], 0\n\t"
          "v_exp_f32 v127, v127\n\t"
          "v_exp_f32 v128, v128\n\t"
          "v_exp_f32 v129, v129\n\t"
          "v_exp_f32 v130, v130\n\t"
          "v_mfma_f32_32x32x16_bf16 v[144:159], %[k1], %[qa16], v[144:159]\n\t"
          "v_exp_f32 v131, v131\n\t"
          "v_exp_f32 v132, v132\n\t"
          "v_exp_f32 v133, v133\n\t"
          "v_mfma_f32_32x32x16_bf16 v[232:247], v[184:187], v[196:199], v[232:247]\n\t"
          "v_exp_f32 v134, v134\n\t"
          "v_exp_f32 v135, v135\n\t"
          "v_exp_f32 v136, v136\n\t"
          "v_smfmac_f32_32x32x32_bf16 %[oa], %[v00], v[112:119], %[idx]\n\t"
          "ds_read2_b64 v[204:207], %[fb] offset0:16 offset1:17\n\t"
          "ds_read_b128 %[k0], %[ak] offset:9152\n\t"
          "v_exp_f32 v137, v137\n\t"
          "v_mfma_f32_32x32x16_bf16 v[216:231], v[184:187], v[192:195], v[216:231]\n\t"
          "v_exp_f32 v138, v138\n\t"
          "v_exp_f32 v139, v139\n\t"
          "v_exp_f32 v140, v140\n\t"
          "v_smfmac_f32_32x32x32_bf16 %[oa], %[v10], v[120:127], %[idx]\n\t"
          "ds_read_b128 %[v01], %[av] offset:7104\n\t"
          "ds_read_b128 %[v11], %[av] offset:7776\n\t"
          "v_exp_f32 v141, v141\n\t"
          "v_exp_f32 v142, v142\n\t"
          "v_mfma_f32_32x32x8_bf16 v[160:175], v[178:179], %[qb8e], 0\n\t"
          "v_exp_f32 v143, v143\n\t"
          "v_exp_f32 v144, v144\n\t"
          "v_exp_f32 v145, v145\n\t"
          "v_exp_f32 v146, v146\n\t"
          "v_mfma_f32_32x32x16_bf16 v[160:175], %[k1], %[qb16], v[160:175]\n\t"
          "s_waitcnt lgkmcnt(6)\n\t"
          "v_exp_f32 v147, v147\n\t"
          "v_exp_f32 v148, v148\n\t"
          "v_exp_f32 v149, v149\n\t"
          "v_mfma_f32_32x32x16_bf16 v[232:247], v[188:191], v[192:195], v[232:247]\n\t"
          "v_exp_f32 v150, v150\n\t"
          "v_exp_f32 v151, v151\n\t"
          "v_exp_f32 v152, v152\n\t"
          "v_smfmac_f32_32x32x32_bf16 %[ob], %[v00], v[128:135], %[idx]\n\t"
          "s_waitcnt lgkmcnt(4)\n\t"
          "ds_read2_b64 v[184:187], %[fa] offset0:28 offset1:29\n\t"
          "ds_read_b128 v[196:199], %[ub] offset:12512\n\t"
          "v_exp_f32 v153, v153\n\t"
          "v_mfma_f32_32x32x16_bf16 v[232:247], v[200:203], v[212:215], v[232:247]\n\t"
          "v_exp_f32 v154, v154\n\t"
          "v_exp_f32 v155, v155\n\t"
          "v_exp_f32 v156, v156\n\t"
          "v_smfmac_f32_32x32x32_bf16 %[ob], %[v10], v[136:143], %[idx]\n\t"
          "s_waitcnt lgkmcnt(2)\n\t"
          "ds_read2_b64 v[188:191], %[fb] offset0:28 offset1:29\n\t"
          "ds_read_b128 v[192:195], %[ub] offset:3808\n\t"
          "v_exp_f32 v157, v157\n\t"
          "v_exp_f32 v158, v158\n\t"
          "v_mfma_f32_32x32x8_bf16 v[112:127], v[178:179], %[qa8o], 0\n\t"
          "v_exp_f32 v159, v159\n\t"
          "v_exp_f32 v160, v160\n\t"
          "v_exp_f32 v161, v161\n\t"
          "v_exp_f32 v162, v162\n\t"
          "v_mfma_f32_32x32x16_bf16 v[112:127], %[k0], %[qa16], v[112:127]\n\t"
          "v_exp_f32 v163, v163\n\t"
          "v_exp_f32 v164, v164\n\t"
          "v_exp_f32 v165, v165\n\t"
          "v_mfma_f32_32x32x16_bf16 v[216:231], v[200:203], v[208:211], v[216:231]\n\t"
          "v_exp_f32 v166, v166\n\t"
          "v_exp_f32 v167, v167\n\t"
          "v_exp_f32 v168, v168\n\t"
          "v_smfmac_f32_32x32x32_bf16 %[oa], %[v01], v[144:151], %[idx]\n\t"
          "ds_read_b128 %[k1], %[ak] offset:11520\n\t"
          "ds_read_b128 v[180:183], %[ak] offset:22016\n\t"
          "v_exp_f32 v169, v169\n\t"
          "v_mfma_f32_32x32x16_bf16 v[232:247], v[204:207], v[208:211], v[232:247]\n\t"
          "v_exp_f32 v170, v170\n\t"
          "v_exp_f32 v171, v171\n\t"
          "v_exp_f32 v172, v172\n\t"
          "v_smfmac_f32_32x32x32_bf16 %[oa], %[v11], v[152:159], %[idx]\n\t"
          "ds_read_b128 v[212:215], %[ub] offset:12640\n\t"
          "ds_read_b128 %[v00], %[av] offset:9472\n\t"
          "ds_read_b128 %[v10], %[av] offset:10144\n\t"
          "v_exp_f32 v173, v173\n\t"
          "v_mfma_f32_32x32x8_bf16 v[128:143], v[178:179], %[qb8o], 0\n\t"
          "v_exp_f32 v174, v174\n\t"
          "v_exp_f32 v175, v175\n\t"
          "v_exp_f32 v112, v112\n\t"
          "v_exp_f32 v113, v113\n\t"
          "v_mfma_f32_32x32x16_bf16 v[128:143], %[k0], %[qb16], v[128:143]\n\t"
          "s_waitcnt lgkmcnt(7)\n\t"
          "v_exp_f32 v114, v114\n\t"
          "v_exp_f32 v115, v115\n\t"
          "v_exp_f32 v116, v116\n\t"
          "v_mfma_f32_32x32x16_bf16 v[232:247], v[184:187], v[196:199], v[232:247]\n\t"
          "v_exp_f32 v117, v117\n\t"
          "v_exp_f32 v118, v118\n\t"
          "v_exp_f32 v119, v119\n\t"
          "v_smfmac_f32_32x32x32_bf16 %[ob], %[v01], v[160:167], %[idx]\n\t"
          "s_waitcnt lgkmcnt(5)\n\t"
          "ds_read_b128 v[208:211], %[ub] offset:3936\n\t"
          "ds_read2_b64 v[200:203], %[fa] offset0:24 offset1:25\n\t"
          "v_exp_f32 v120, v120\n\t"
          "v_mfma_f32_32x32x16_bf16 v[216:231], v[184:187], v[192:195], v[216:231]\n\t"
          "v_exp_f32 v121, v121\n\t"
          "v_exp_f32 v122, v122\n\t"
          "v_exp_f32 v123, v123\n\t"
          "v_smfmac_f32_32x32x32_bf16 %[ob], %[v11], v[168:175], %[idx]\n\t"
          "s_waitcnt lgkmcnt(2)\n\t"
          "ds_read_b128 v[196:199], %[ub] offset:12240\n\t"
          "ds_read2_b64 v[204:207], %[fb] offset0:24 offset1:25\n\t"
          "v_exp_f32 v124, v124\n\t"
          "v_exp_f32 v125, v125\n\t"
          "v_mfma_f32_32x32x8_bf16 v[144:159], v[180:181], %[qa8e], 0\n\t"
          "v_exp_f32 v126, v126\n\t"
          "v_exp_f32 v127, v127\n\t"
          "v_exp_f32 v128, v128\n\t"
          "v_exp_f32 v129, v129\n\t"
          "v_mfma_f32_32x32x16_bf16 v[144:159], %[k1], %[qa16], v[144:159]\n\t"
          "v_exp_f32 v130, v130\n\t"
          "v_exp_f32 v131, v131\n\t"
          "v_exp_f32 v132, v132\n\t"
          "v_mfma_f32_32x32x16_bf16 v[232:247], v[188:191], v[192:195], v[232:247]\n\t"
          "v_exp_f32 v133, v133\n\t"
          "v_exp_f32 v134, v134\n\t"
          "v_exp_f32 v135, v135\n\t"
          "v_smfmac_f32_32x32x32_bf16 %[oa], %[v00], v[112:119], %[idx]\n\t"
          "s_waitcnt lgkmcnt(2)\n\t"
          "ds_read2_b64 v[184:187], %[fa] offset0:36 offset1:37\n\t"
          "ds_read_b128 %[k0], %[ak] offset:13888\n\t"
          "v_exp_f32 v136, v136\n\t"
          "v_mfma_f32_32x32x16_bf16 v[232:247], v[200:203], v[212:215], v[232:247]\n\t"
          "v_exp_f32 v137, v137\n\t"
          "v_exp_f32 v138, v138\n\t"
          "v_exp_f32 v139, v139\n\t"
          "v_smfmac_f32_32x32x32_bf16 %[oa], %[v10], v[120:127], %[idx]\n\t"
          "ds_read_b128 v[192:195], %[ub] offset:3536\n\t"
          "ds_read_b128 %[v01], %[av] offset:11840\n\t"
          "ds_read_b128 %[v11], %[av] offset:12512\n\t"
          "v_exp_f32 v140, v140\n\t"
          "v_exp_f32 v141, v141\n\t"
          "v_mfma_f32_32x32x8_bf16 v[160:175], v[180:181], %[qb8e], 0\n\t"
          "v_exp_f32 v142, v142\n\t"
          "v_exp_f32 v143, v143\n\t"
          "v_exp_f32 v144, v144\n\t"
          "v_exp_f32 v145, v145\n\t"
          "v_mfma_f32_32x32x16_bf16 v[160:175], %[k1], %[qb16], v[160:175]\n\t"
          "v_exp_f32 v146, v146\n\t"
          "v_exp_f32 v147, v147\n\t"
          "v_exp_f32 v148, v148\n\t"
          "v_mfma_f32_32x32x16_bf16 v[216:231], v[200:203], v[208:211], v[216:231]\n\t"
          "v_exp_f32 v149, v149\n\t"
          "v_exp_f32 v150, v150\n\t"
          "v_exp_f32 v151, v151\n\t"
          "v_smfmac_f32_32x32x32_bf16 %[ob], %[v00], v[128:135], %[idx]\n\t"
          "s_waitcnt lgkmcnt(5)\n\t"
          "ds_read_b128 v[212:215], %[ub] offset:12368\n\t"
          "ds_read2_b64 v[188:191], %[fb] offset0:36 offset1:37\n\t"
          "v_exp_f32 v152, v152\n\t"
          "v_mfma_f32_32x32x16_bf16 v[232:247], v[204:207], v[208:211], v[232:247]\n\t"
          "v_exp_f32 v153, v153\n\t"
          "v_exp_f32 v154, v154\n\t"
          "v_exp_f32 v155, v155\n\t"
          "v_smfmac_f32_32x32x32_bf16 %[ob], %[v10], v[136:143], %[idx]\n\t"
          "s_waitcnt lgkmcnt(2)\n\t"
          "ds_read_b128 v[208:211], %[ub] offset:3664\n\t"
          "ds_read2_b64 v[200:203], %[fa] offset0:32 offset1:33\n\t"
          "v_exp_f32 v156, v156\n\t"
          "v_exp_f32 v157, v157\n\t"
          "v_mfma_f32_32x32x8_bf16 v[112:127], v[180:181], %[qa8o], 0\n\t"
          "v_exp_f32 v158, v158\n\t"
          "v_exp_f32 v159, v159\n\t"
          "v_exp_f32 v160, v160\n\t"
          "v_exp_f32 v161, v161\n\t"
          "v_mfma_f32_32x32x16_bf16 v[112:127], %[k0], %[qa16], v[112:127]\n\t"
          "v_exp_f32 v162, v162\n\t"
          "v_exp_f32 v163, v163\n\t"
          "v_exp_f32 v164, v164\n\t"
          "v_mfma_f32_32x32x16_bf16 v[232:247], v[184:187], v[196:199], v[232:247]\n\t"
          "v_exp_f32 v165, v165\n\t"
          "v_exp_f32 v166, v166\n\t"
          "v_exp_f32 v167, v167\n\t"
          "v_smfmac_f32_32x32x32_bf16 %[oa], %[v01], v[144:151], %[idx]\n\t"
          "ds_read2_b64 v[204:207], %[fb] offset0:32 offset1:33\n\t"
          "ds_read_b128 %[k1], %[ak] offset:16256\n\t"
          "v_exp_f32 v168, v168\n\t"
          "v_mfma_f32_32x32x16_bf16 v[216:231], v[184:187], v[192:195], v[216:231]\n\t"
          "v_exp_f32 v169, v169\n\t"
          "v_exp_f32 v170, v170\n\t"
          "v_exp_f32 v171, v171\n\t"
          "v_smfmac_f32_32x32x32_bf16 %[oa], %[v11], v[152:159], %[idx]\n\t"
          "ds_read_b128 %[v00], %[av] offset:14208\n\t"
          "ds_read_b128 %[v10], %[av] offset:14880\n\t"
          "v_exp_f32 v172, v172\n\t"
          "v_exp_f32 v173, v173\n\t"
          "v_mfma_f32_32x32x8_bf16 v[128:143], v[180:181], %[qb8o], 0\n\t"
          "v_exp_f32 v174, v174\n\t"
          "v_exp_f32 v175, v175\n\t"
          "v_exp_f32 v112, v112\n\t"
          "v_exp_f32 v113, v113\n\t"
          "v_mfma_f32_32x32x16_bf16 v[128:143], %[k0], %[qb16], v[128:143]\n\t"
          "s_waitcnt lgkmcnt(6)\n\t"
          "v_exp_f32 v114, v114\n\t"
          "v_exp_f32 v115, v115\n\t"
          "v_exp_f32 v116, v116\n\t"
          "v_mfma_f32_32x32x16_bf16 v[232:247], v[188:191], v[192:195], v[232:247]\n\t"
          "v_exp_f32 v117, v117\n\t"
          "v_exp_f32 v118, v118\n\t"
          "v_exp_f32 v119, v119\n\t"
          "v_smfmac_f32_32x32x32_bf16 %[ob], %[v01], v[160:167], %[idx]\n\t"
          "s_waitcnt lgkmcnt(4)\n\t"
          "ds_read2_b64 v[184:187], %[fa] offset0:44 offset1:45\n\t"
          "ds_read_b128 v[196:199], %[ub] offset:11968\n\t"
          "v_exp_f32 v120, v120\n\t"
          "v_mfma_f32_32x32x16_bf16 v[232:247], v[200:203], v[212:215], v[232:247]\n\t"
          "v_exp_f32 v121, v121\n\t"
          "v_exp_f32 v122, v122\n\t"
          "v_exp_f32 v123, v123\n\t"
          "v_smfmac_f32_32x32x32_bf16 %[ob], %[v11], v[168:175], %[idx]\n\t"
          "s_waitcnt lgkmcnt(2)\n\t"
          "ds_read2_b64 v[188:191], %[fb] offset0:44 offset1:45\n\t"
          "ds_read_b128 v[192:195], %[ub] offset:3264\n\t"
          "v_exp_f32 v124, v124\n\t"
          "v_exp_f32 v125, v125\n\t"
          "v_mfma_f32_32x32x8_bf16 v[144:159], v[182:183], %[qa8e], 0\n\t"
          "v_exp_f32 v126, v126\n\t"
          "v_exp_f32 v127, v127\n\t"
          "v_exp_f32 v128, v128\n\t"
          "v_exp_f32 v129, v129\n\t"
          "v_mfma_f32_32x32x16_bf16 v[144:159], %[k1], %[qa16], v[144:159]\n\t"
          "v_exp_f32 v130, v130\n\t"
          "v_exp_f32 v131, v131\n\t"
          "v_exp_f32 v132, v132\n\t"
          "v_mfma_f32_32x32x16_bf16 v[216:231], v[200:203], v[208:211], v[216:231]\n\t"
          "v_exp_f32 v133, v133\n\t"
          "v_exp_f32 v134, v134\n\t"
          "v_exp_f32 v135, v135\n\t"
          "v_smfmac_f32_32x32x32_bf16 %[oa], %[v00], v[112:119], %[idx]\n\t"
          "ds_read_b128 v[212:215], %[ub] offset:12096\n\t"
          "ds_read_b128 %[k0], %[ak] offset:18624\n\t"
          "v_exp_f32 v136, v136\n\t"
          "v_exp_f32 v137, v137\n\t"
          "v_mfma_f32_32x32x16_bf16 v[232:247], v[204:207], v[208:211], v[232:247]\n\t"
          "v_exp_f32 v138, v138\n\t"
          "v_exp_f32 v139, v139\n\t"
          "v_exp_f32 v140, v140\n\t"
          "v_smfmac_f32_32x32x32_bf16 %[oa], %[v10], v[120:127], %[idx]\n\t"
          "ds_read2_b64 v[200:203], %[fa] offset0:40 offset1:41\n\t"
          "ds_read_b128 %[v01], %[av] offset:16576\n\t"
          "ds_read_b128 %[v11], %[av] offset:17248\n\t"
          "v_exp_f32 v141, v141\n\t"
          "v_exp_f32 v142, v142\n\t"
          "v_mfma_f32_32x32x8_bf16 v[160:175], v[182:183], %[qb8e], 0\n\t"
          "v_exp_f32 v143, v143\n\t"
          "v_exp_f32 v144, v144\n\t"
          "v_exp_f32 v145, v145\n\t"
          "v_exp_f32 v146, v146\n\t"
          "v_mfma_f32_32x32x16_bf16 v[160:175], %[k1], %[qb16], v[160:175]\n\t"
          "s_waitcnt lgkmcnt(7)\n\t"
          "v_add_u32 %[ak], %[dl], %[ak]\n\t"
          "v_add_u32 %[av], %[dl], %[av]\n\t"
          "v_exp_f32 v147, v147\n\t"
          "v_exp_f32 v148, v148\n\t"
          "v_mfma_f32_32x32x16_bf16 v[232:247], v[184:187], v[196:199], v[232:247]\n\t"
          "v_exp_f32 v149, v149\n\t"
          "v_exp_f32 v150, v150\n\t"
          "v_exp_f32 v151, v151\n\t"
          "v_smfmac_f32_32x32x32_bf16 %[ob], %[v00], v[128:135], %[idx]\n\t"
          "s_waitcnt lgkmcnt(5)\n\t"
          "ds_read2_b64 v[204:207], %[fb] offset0:40 offset1:41\n\t"
          "ds_read_b128 v[208:211], %[ub] offset:3392\n\t"
          "s_mul_i32 %[dl], %[dl], -1\n\t"
          "v_exp_f32 v152, v152\n\t"
          "v_mfma_f32_32x32x16_bf16 v[216:231], v[184:187], v[192:195], v[216:231]\n\t"
          "v_exp_f32 v153, v153\n\t"
          "v_exp_f32 v154, v154\n\t"
          "v_exp_f32 v155, v155\n\t"
          "v_smfmac_f32_32x32x32_bf16 %[ob], %[v10], v[136:143], %[idx]\n\t"
          "s_waitcnt lgkmcnt(2)\n\t"
          "ds_read2_b64 v[184:187], %[fa] offset0:52 offset1:53\n\t"
          "ds_read_b128 v[196:199], %[ub] offset:11696\n\t"
          "s_barrier\n\t"
          "v_exp_f32 v156, v156\n\t"
          "v_exp_f32 v157, v157\n\t"
          "v_mfma_f32_32x32x8_bf16 v[112:127], v[182:183], %[qa8o], 0\n\t"
          "v_exp_f32 v158, v158\n\t"
          "v_exp_f32 v159, v159\n\t"
          "v_exp_f32 v160, v160\n\t"
          "v_exp_f32 v161, v161\n\t"
          "v_mfma_f32_32x32x16_bf16 v[112:127], %[k0], %[qa16], v[112:127]\n\t"
          "v_exp_f32 v162, v162\n\t"
          "v_exp_f32 v163, v163\n\t"
          "v_exp_f32 v164, v164\n\t"
          "v_mfma_f32_32x32x16_bf16 v[232:247], v[188:191], v[192:195], v[232:247]\n\t"
          "v_exp_f32 v165, v165\n\t"
          "v_exp_f32 v166, v166\n\t"
          "v_exp_f32 v167, v167\n\t"
          "v_smfmac_f32_32x32x32_bf16 %[oa], %[v01], v[144:151], %[idx]\n\t"
          "ds_read_b128 %[k1], %[ak] offset:2048\n\t"
          "ds_read_b128 v[176:179], %[ak] offset:20992\n\t"
          "v_exp_f32 v168, v168\n\t"
          "v_mfma_f32_32x32x16_bf16 v[232:247], v[200:203], v[212:215], v[232:247]\n\t"
          "v_exp_f32 v169, v169\n\t"
          "v_exp_f32 v170, v170\n\t"
          "v_exp_f32 v171, v171\n\t"
          "v_smfmac_f32_32x32x32_bf16 %[oa], %[v11], v[152:159], %[idx]\n\t"
          "ds_read_b128 v[192:195], %[ub] offset:2992\n\t"
          "ds_read_b128 %[v00], %[av] offset:0\n\t"
          "ds_read_b128 %[v10], %[av] offset:672\n\t"
          "v_exp_f32 v172, v172\n\t"
          "v_exp_f32 v173, v173\n\t"
          "v_mfma_f32_32x32x8_bf16 v[128:143], v[182:183], %[qb8o], 0\n\t"
          "v_exp_f32 v174, v174\n\t"
          "v_exp_f32 v175, v175\n\t"
          "v_exp_f32 v112, v112\n\t"
          "v_exp_f32 v113, v113\n\t"
          "v_mfma_f32_32x32x16_bf16 v[128:143], %[k0], %[qb16], v[128:143]\n\t"
          "s_waitcnt lgkmcnt(7)\n\t"
          "v_exp_f32 v114, v114\n\t"
          "v_exp_f32 v115, v115\n\t"
          "v_exp_f32 v116, v116\n\t"
          "v_mfma_f32_32x32x16_bf16 v[216:231], v[200:203], v[208:211], v[216:231]\n\t"
          "v_exp_f32 v117, v117\n\t"
          "v_exp_f32 v118, v118\n\t"
          "v_exp_f32 v119, v119\n\t"
          "v_smfmac_f32_32x32x32_bf16 %[ob], %[v01], v[160:167], %[idx]\n\t"
          "ds_read_b128 v[212:215], %[ub] offset:11824\n\t"
          "ds_read2_b64 v[188:191], %[fb] offset0:52 offset1:53\n\t"
          "v_exp_f32 v120, v120\n\t"
          "v_mfma_f32_32x32x16_bf16 v[232:247], v[204:207], v[208:211], v[232:247]\n\t"
          "v_exp_f32 v121, v121\n\t"
          "v_exp_f32 v122, v122\n\t"
          "v_exp_f32 v123, v123\n\t"
          "v_smfmac_f32_32x32x32_bf16 %[ob], %[v11], v[168:175], %[idx]\n\t"
          "s_waitcnt lgkmcnt(2)\n\t"
          "ds_read_b128 v[208:211], %[ub] offset:3120\n\t"
          "ds_read2_b64 v[200:203], %[fa] offset0:48 offset1:49\n\t"
          "v_exp_f32 v124, v124\n\t"
          "v_exp_f32 v125, v125\n\t"
          "v_mfma_f32_32x32x8_bf16 v[144:159], v[176:177], %[qa8e], 0\n\t"
          "v_exp_f32 v126, v126\n\t"
          "v_exp_f32 v127, v127\n\t"
          "v_exp_f32 v128, v128\n\t"
          "v_exp_f32 v129, v129\n\t"
          "v_mfma_f32_32x32x16_bf16 v[144:159], %[k1], %[qa16], v[144:159]\n\t"
          "v_exp_f32 v130, v130\n\t"
          "v_exp_f32 v131, v131\n\t"
          "v_exp_f32 v132, v132\n\t"
          "v_mfma_f32_32x32x16_bf16 v[232:247], v[184:187], v[196:199], v[232:247]\n\t"
          "v_exp_f32 v133, v133\n\t"
          "v_exp_f32 v134, v134\n\t"
          "v_exp_f32 v135, v135\n\t"
          "v_smfmac_f32_32x32x32_bf16 %[oa], %[v00], v[112:119], %[idx]\n\t"
          "ds_read2_b64 v[204:207], %[fb] offset0:48 offset1:49\n\t"
          "ds_read_b128 %[k0], %[ak] offset:4416\n\t"
          "v_exp_f32 v136, v136\n\t"
          "v_mfma_f32_32x32x16_bf16 v[216:231], v[184:187], v[192:195], v[216:231]\n\t"
          "v_exp_f32 v137, v137\n\t"
          "v_exp_f32 v138, v138\n\t"
          "v_exp_f32 v139, v139\n\t"
          "v_smfmac_f32_32x32x32_bf16 %[oa], %[v10], v[120:127], %[idx]\n\t"
          "ds_read_b128 %[v01], %[av] offset:2368\n\t"
          "ds_read_b128 %[v11], %[av] offset:3040\n\t"
          "v_exp_f32 v140, v140\n\t"
          "v_exp_f32 v141, v141\n\t"
          "v_mfma_f32_32x32x8_bf16 v[160:175], v[176:177], %[qb8e], 0\n\t"
          "v_exp_f32 v142, v142\n\t"
          "v_exp_f32 v143, v143\n\t"
          "v_exp_f32 v144, v144\n\t"
          "v_exp_f32 v145, v145\n\t"
          "v_mfma_f32_32x32x16_bf16 v[160:175], %[k1], %[qb16], v[160:175]\n\t"
          "s_waitcnt lgkmcnt(6)\n\t"
          "v_exp_f32 v146, v146\n\t"
          "v_exp_f32 v147, v147\n\t"
          "v_exp_f32 v148, v148\n\t"
          "v_mfma_f32_32x32x16_bf16 v[232:247], v[188:191], v[192:195], v[232:247]\n\t"
          "v_exp_f32 v149, v149\n\t"
          "v_exp_f32 v150, v150\n\t"
          "v_exp_f32 v151, v151\n\t"
          "v_smfmac_f32_32x32x32_bf16 %[ob], %[v00], v[128:135], %[idx]\n\t"
          "s_waitcnt lgkmcnt(4)\n\t"
          "ds_read2_b64 v[184:187], %[fa] offset0:60 offset1:61\n\t"
          "ds_read_b128 v[196:199], %[ub] offset:11424\n\t"
          "v_exp_f32 v152, v152\n\t"
          "v_mfma_f32_32x32x16_bf16 v[232:247], v[200:203], v[212:215], v[232:247]\n\t"
          "v_exp_f32 v153, v153\n\t"
          "v_exp_f32 v154, v154\n\t"
          "v_exp_f32 v155, v155\n\t"
          "v_smfmac_f32_32x32x32_bf16 %[ob], %[v10], v[136:143], %[idx]\n\t"
          "s_waitcnt lgkmcnt(2)\n\t"
          "ds_read2_b64 v[188:191], %[fb] offset0:60 offset1:61\n\t"
          "ds_read_b128 v[192:195], %[ub] offset:2720\n\t"
          "v_exp_f32 v156, v156\n\t"
          "v_exp_f32 v157, v157\n\t"
          "v_mfma_f32_32x32x8_bf16 v[112:127], v[176:177], %[qa8o], 0\n\t"
          "v_exp_f32 v158, v158\n\t"
          "v_exp_f32 v159, v159\n\t"
          "v_exp_f32 v160, v160\n\t"
          "v_exp_f32 v161, v161\n\t"
          "v_mfma_f32_32x32x16_bf16 v[112:127], %[k0], %[qa16], v[112:127]\n\t"
          "v_exp_f32 v162, v162\n\t"
          "v_exp_f32 v163, v163\n\t"
          "v_exp_f32 v164, v164\n\t"
          "v_mfma_f32_32x32x16_bf16 v[216:231], v[200:203], v[208:211], v[216:231]\n\t"
          "v_exp_f32 v165, v165\n\t"
          "v_exp_f32 v166, v166\n\t"
          "v_exp_f32 v167, v167\n\t"
          "v_smfmac_f32_32x32x32_bf16 %[oa], %[v01], v[144:151], %[idx]\n\t"
          "ds_read_b128 v[212:215], %[ub] offset:11552\n\t"
          "ds_read_b128 %[k1], %[ak] offset:6784\n\t"
          "v_exp_f32 v168, v168\n\t"
          "v_mfma_f32_32x32x16_bf16 v[232:247], v[204:207], v[208:211], v[232:247]\n\t"
          "v_exp_f32 v169, v169\n\t"
          "v_exp_f32 v170, v170\n\t"
          "v_exp_f32 v171, v171\n\t"
          "v_smfmac_f32_32x32x32_bf16 %[oa], %[v11], v[152:159], %[idx]\n\t"
          "ds_read2_b64 v[200:203], %[fa] offset0:56 offset1:57\n\t"
          "ds_read_b128 %[v00], %[av] offset:4736\n\t"
          "ds_read_b128 %[v10], %[av] offset:5408\n\t"
          "v_exp_f32 v172, v172\n\t"
          "v_exp_f32 v173, v173\n\t"
          "v_mfma_f32_32x32x8_bf16 v[128:143], v[176:177], %[qb8o], 0\n\t"
          "v_exp_f32 v174, v174\n\t"
          "v_exp_f32 v175, v175\n\t"
          "v_exp_f32 v112, v112\n\t"
          "v_exp_f32 v113, v113\n\t"
          "v_mfma_f32_32x32x16_bf16 v[128:143], %[k0], %[qb16], v[128:143]\n\t"
          "s_waitcnt lgkmcnt(7)\n\t"
          "v_exp_f32 v114, v114\n\t"
          "v_exp_f32 v115, v115\n\t"
          "v_exp_f32 v116, v116\n\t"
          "v_mfma_f32_32x32x16_bf16 v[232:247], v[184:187], v[196:199], v[232:247]\n\t"
          "v_exp_f32 v117, v117\n\t"
          "v_exp_f32 v118, v118\n\t"
          "v_exp_f32 v119, v119\n\t"
          "v_smfmac_f32_32x32x32_bf16 %[ob], %[v01], v[160:167], %[idx]\n\t"
          "s_waitcnt lgkmcnt(5)\n\t"
          "ds_read2_b64 v[204:207], %[fb] offset0:56 offset1:57\n\t"
          "ds_read_b128 v[208:211], %[ub] offset:2848\n\t"
          "v_exp_f32 v120, v120\n\t"
          "v_mfma_f32_32x32x16_bf16 v[216:231], v[184:187], v[192:195], v[216:231]\n\t"
          "v_exp_f32 v121, v121\n\t"
          "v_exp_f32 v122, v122\n\t"
          "v_exp_f32 v123, v123\n\t"
          "v_smfmac_f32_32x32x32_bf16 %[ob], %[v11], v[168:175], %[idx]\n\t"
          "s_waitcnt lgkmcnt(2)\n\t"
          "ds_read2_b64 v[184:187], %[fa] offset0:68 offset1:69\n\t"
          "ds_read_b128 v[196:199], %[ub] offset:11152\n\t"
          "v_exp_f32 v124, v124\n\t"
          "v_exp_f32 v125, v125\n\t"
          "v_mfma_f32_32x32x8_bf16 v[144:159], v[178:179], %[qa8e], 0\n\t"
          "v_exp_f32 v126, v126\n\t"
          "v_exp_f32 v127, v127\n\t"
          "v_exp_f32 v128, v128\n\t"
          "v_exp_f32 v129, v129\n\t"
          "v_mfma_f32_32x32x16_bf16 v[144:159], %[k1], %[qa16], v[144:159]\n\t"
          "v_exp_f32 v130, v130\n\t"
          "v_exp_f32 v131, v131\n\t"
          "v_exp_f32 v132, v132\n\t"
          "v_mfma_f32_32x32x16_bf16 v[232:247], v[188:191], v[192:195], v[232:247]\n\t"
          "v_exp_f32 v133, v133\n\t"
          "v_exp_f32 v134, v134\n\t"
          "v_exp_f32 v135, v135\n\t"
          "v_smfmac_f32_32x32x32_bf16 %[oa], %[v00], v[112:119], %[idx]\n\t"
          "ds_read_b128 v[192:195], %[ub] offset:2448\n\t"
          "ds_read_b128 %[k0], %[ak] offset:9152\n\t"
          "v_exp_f32 v136, v136\n\t"
          "v_mfma_f32_32x32x16_bf16 v[232:247], v[200:203], v[212:215], v[232:247]\n\t"
          "v_exp_f32 v137, v137\n\t"
          "v_exp_f32 v138, v138\n\t"
          "v_exp_f32 v139, v139\n\t"
          "v_smfmac_f32_32x32x32_bf16 %[oa], %[v10], v[120:127], %[idx]\n\t"
          "ds_read_b128 %[v01], %[av] offset:7104\n\t"
          "ds_read_b128 %[v11], %[av] offset:7776\n\t"
          "v_exp_f32 v140, v140\n\t"
          "v_exp_f32 v141, v141\n\t"
          "v_mfma_f32_32x32x8_bf16 v[160:175], v[178:179], %[qb8e], 0\n\t"
          "v_exp_f32 v142, v142\n\t"
          "v_exp_f32 v143, v143\n\t"
          "v_exp_f32 v144, v144\n\t"
          "v_exp_f32 v145, v145\n\t"
          "v_mfma_f32_32x32x16_bf16 v[160:175], %[k1], %[qb16], v[160:175]\n\t"
          "s_waitcnt lgkmcnt(6)\n\t"
          "v_exp_f32 v146, v146\n\t"
          "v_exp_f32 v147, v147\n\t"
          "v_exp_f32 v148, v148\n\t"
          "v_mfma_f32_32x32x16_bf16 v[216:231], v[200:203], v[208:211], v[216:231]\n\t"
          "v_exp_f32 v149, v149\n\t"
          "v_exp_f32 v150, v150\n\t"
          "v_exp_f32 v151, v151\n\t"
          "v_smfmac_f32_32x32x32_bf16 %[ob], %[v00], v[128:135], %[idx]\n\t"
          "ds_read_b128 v[212:215], %[ub] offset:11280\n\t"
          "ds_read2_b64 v[188:191], %[fb] offset0:68 offset1:69\n\t"
          "v_exp_f32 v152, v152\n\t"
          "v_mfma_f32_32x32x16_bf16 v[232:247], v[204:207], v[208:211], v[232:247]\n\t"
          "v_exp_f32 v153, v153\n\t"
          "v_exp_f32 v154, v154\n\t"
          "v_exp_f32 v155, v155\n\t"
          "v_smfmac_f32_32x32x32_bf16 %[ob], %[v10], v[136:143], %[idx]\n\t"
          "s_waitcnt lgkmcnt(2)\n\t"
          "ds_read_b128 v[208:211], %[ub] offset:2576\n\t"
          "ds_read2_b64 v[200:203], %[fa] offset0:64 offset1:65\n\t"
          "v_exp_f32 v156, v156\n\t"
          "v_exp_f32 v157, v157\n\t"
          "v_mfma_f32_32x32x8_bf16 v[112:127], v[178:179], %[qa8o], 0\n\t"
          "v_exp_f32 v158, v158\n\t"
          "v_exp_f32 v159, v159\n\t"
          "v_exp_f32 v160, v160\n\t"
          "v_exp_f32 v161, v161\n\t"
          "v_mfma_f32_32x32x16_bf16 v[112:127], %[k0], %[qa16], v[112:127]\n\t"
          "v_exp_f32 v162, v162\n\t"
          "v_exp_f32 v163, v163\n\t"
          "v_exp_f32 v164, v164\n\t"
          "v_mfma_f32_32x32x16_bf16 v[232:247], v[184:187], v[196:199], v[232:247]\n\t"
          "v_exp_f32 v165, v165\n\t"
          "v_exp_f32 v166, v166\n\t"
          "v_exp_f32 v167, v167\n\t"
          "v_smfmac_f32_32x32x32_bf16 %[oa], %[v01], v[144:151], %[idx]\n\t"
          "ds_read_b128 %[k1], %[ak] offset:11520\n\t"
          "ds_read_b128 v[180:183], %[ak] offset:22016\n\t"
          "v_exp_f32 v168, v168\n\t"
          "v_mfma_f32_32x32x16_bf16 v[216:231], v[184:187], v[192:195], v[216:231]\n\t"
          "v_exp_f32 v169, v169\n\t"
          "v_exp_f32 v170, v170\n\t"
          "v_exp_f32 v171, v171\n\t"
          "v_smfmac_f32_32x32x32_bf16 %[oa], %[v11], v[152:159], %[idx]\n\t"
          "ds_read2_b64 v[204:207], %[fb] offset0:64 offset1:65\n\t"
          "ds_read_b128 %[v00], %[av] offset:9472\n\t"
          "ds_read_b128 %[v10], %[av] offset:10144\n\t"
          "v_exp_f32 v172, v172\n\t"
          "v_exp_f32 v173, v173\n\t"
          "v_mfma_f32_32x32x8_bf16 v[128:143], v[178:179], %[qb8o], 0\n\t"
          "v_exp_f32 v174, v174\n\t"
          "v_exp_f32 v175, v175\n\t"
          "v_exp_f32 v112, v112\n\t"
          "v_exp_f32 v113, v113\n\t"
          "v_mfma_f32_32x32x16_bf16 v[128:143], %[k0], %[qb16], v[128:143]\n\t"
          "s_waitcnt lgkmcnt(7)\n\t"
          "v_exp_f32 v114, v114\n\t"
          "v_exp_f32 v115, v115\n\t"
          "v_exp_f32 v116, v116\n\t"
          "v_mfma_f32_32x32x16_bf16 v[232:247], v[188:191], v[192:195], v[232:247]\n\t"
          "v_exp_f32 v117, v117\n\t"
          "v_exp_f32 v118, v118\n\t"
          "v_exp_f32 v119, v119\n\t"
          "v_smfmac_f32_32x32x32_bf16 %[ob], %[v01], v[160:167], %[idx]\n\t"
          "s_waitcnt lgkmcnt(5)\n\t"
          "ds_read2_b64 v[184:187], %[fa] offset0:76 offset1:77\n\t"
          "ds_read_b128 v[196:199], %[ub] offset:10880\n\t"
          "v_exp_f32 v120, v120\n\t"
          "v_mfma_f32_32x32x16_bf16 v[232:247], v[200:203], v[212:215], v[232:247]\n\t"
          "v_exp_f32 v121, v121\n\t"
          "v_exp_f32 v122, v122\n\t"
          "v_exp_f32 v123, v123\n\t"
          "v_smfmac_f32_32x32x32_bf16 %[ob], %[v11], v[168:175], %[idx]\n\t"
          "s_waitcnt lgkmcnt(2)\n\t"
          "ds_read2_b64 v[188:191], %[fb] offset0:76 offset1:77\n\t"
          "ds_read_b128 v[192:195], %[ub] offset:2176\n\t"
          "v_exp_f32 v124, v124\n\t"
          "v_exp_f32 v125, v125\n\t"
          "v_mfma_f32_32x32x8_bf16 v[144:159], v[180:181], %[qa8e], 0\n\t"
          "v_exp_f32 v126, v126\n\t"
          "v_exp_f32 v127, v127\n\t"
          "v_exp_f32 v128, v128\n\t"
          "v_exp_f32 v129, v129\n\t"
          "v_mfma_f32_32x32x16_bf16 v[144:159], %[k1], %[qa16], v[144:159]\n\t"
          "v_exp_f32 v130, v130\n\t"
          "v_exp_f32 v131, v131\n\t"
          "v_exp_f32 v132, v132\n\t"
          "v_mfma_f32_32x32x16_bf16 v[216:231], v[200:203], v[208:211], v[216:231]\n\t"
          "v_exp_f32 v133, v133\n\t"
          "v_exp_f32 v134, v134\n\t"
          "v_exp_f32 v135, v135\n\t"
          "v_smfmac_f32_32x32x32_bf16 %[oa], %[v00], v[112:119], %[idx]\n\t"
          "ds_read_b128 v[212:215], %[ub] offset:11008\n\t"
          "ds_read_b128 %[k0], %[ak] offset:13888\n\t"
          "v_exp_f32 v136, v136\n\t"
          "v_mfma_f32_32x32x16_bf16 v[232:247], v[204:207], v[208:211], v[232:247]\n\t"
          "v_exp_f32 v137, v137\n\t"
          "v_exp_f32 v138, v138\n\t"
          "v_exp_f32 v139, v139\n\t"
          "v_smfmac_f32_32x32x32_bf16 %[oa], %[v10], v[120:127], %[idx]\n\t"
          "ds_read2_b64 v[200:203], %[fa] offset0:72 offset1:73\n\t"
          "ds_read_b128 %[v01], %[av] offset:11840\n\t"
          "ds_read_b128 %[v11], %[av] offset:12512\n\t"
          "v_exp_f32 v140, v140\n\t"
          "v_exp_f32 v141, v141\n\t"
          "v_mfma_f32_32x32x8_bf16 v[160:175], v[180:181], %[qb8e], 0\n\t"
          "v_exp_f32 v142, v142\n\t"
          "v_exp_f32 v143, v143\n\t"
          "v_exp_f32 v144, v144\n\t"
          "v_exp_f32 v145, v145\n\t"
          "v_mfma_f32_32x32x16_bf16 v[160:175], %[k1], %[qb16], v[160:175]\n\t"
          "s_waitcnt lgkmcnt(7)\n\t"
          "v_exp_f32 v146, v146\n\t"
          "v_exp_f32 v147, v147\n\t"
          "v_exp_f32 v148, v148\n\t"
          "v_mfma_f32_32x32x16_bf16 v[232:247], v[184:187], v[196:199], v[232:247]\n\t"
          "v_exp_f32 v149, v149\n\t"
          "v_exp_f32 v150, v150\n\t"
          "v_exp_f32 v151, v151\n\t"
          "v_smfmac_f32_32x32x32_bf16 %[ob], %[v00], v[128:135], %[idx]\n\t"
          "s_waitcnt lgkmcnt(5)\n\t"
          "ds_read2_b64 v[204:207], %[fb] offset0:72 offset1:73\n\t"
          "ds_read_b128 v[208:211], %[ub] offset:2304\n\t"
          "v_exp_f32 v152, v152\n\t"
          "v_mfma_f32_32x32x16_bf16 v[216:231], v[184:187], v[192:195], v[216:231]\n\t"
          "v_exp_f32 v153, v153\n\t"
          "v_exp_f32 v154, v154\n\t"
          "v_exp_f32 v155, v155\n\t"
          "v_smfmac_f32_32x32x32_bf16 %[ob], %[v10], v[136:143], %[idx]\n\t"
          "s_waitcnt lgkmcnt(2)\n\t"
          "ds_read2_b64 v[184:187], %[fa] offset0:84 offset1:85\n\t"
          "ds_read_b128 v[196:199], %[ub] offset:10608\n\t"
          "v_exp_f32 v156, v156\n\t"
          "v_exp_f32 v157, v157\n\t"
          "v_mfma_f32_32x32x8_bf16 v[112:127], v[180:181], %[qa8o], 0\n\t"
          "v_exp_f32 v158, v158\n\t"
          "v_exp_f32 v159, v159\n\t"
          "v_exp_f32 v160, v160\n\t"
          "v_exp_f32 v161, v161\n\t"
          "v_mfma_f32_32x32x16_bf16 v[112:127], %[k0], %[qa16], v[112:127]\n\t"
          "v_exp_f32 v162, v162\n\t"
          "v_exp_f32 v163, v163\n\t"
          "v_exp_f32 v164, v164\n\t"
          "v_mfma_f32_32x32x16_bf16 v[232:247], v[188:191], v[192:195], v[232:247]\n\t"
          "v_exp_f32 v165, v165\n\t"
          "v_exp_f32 v166, v166\n\t"
          "v_exp_f32 v167, v167\n\t"
          "v_smfmac_f32_32x32x32_bf16 %[oa], %[v01], v[144:151], %[idx]\n\t"
          "ds_read_b128 v[192:195], %[ub] offset:1904\n\t"
          "ds_read_b128 %[k1], %[ak] offset:16256\n\t"
          "v_exp_f32 v168, v168\n\t"
          "v_mfma_f32_32x32x16_bf16 v[232:247], v[200:203], v[212:215], v[232:247]\n\t"
          "v_exp_f32 v169, v169\n\t"
          "v_exp_f32 v170, v170\n\t"
          "v_exp_f32 v171, v171\n\t"
          "v_smfmac_f32_32x32x32_bf16 %[oa], %[v11], v[152:159], %[idx]\n\t"
          "ds_read_b128 %[v00], %[av] offset:14208\n\t"
          "ds_read_b128 %[v10], %[av] offset:14880\n\t"
          "v_exp_f32 v172, v172\n\t"
          "v_exp_f32 v173, v173\n\t"
          "v_mfma_f32_32x32x8_bf16 v[128:143], v[180:181], %[qb8o], 0\n\t"
          "v_exp_f32 v174, v174\n\t"
          "v_exp_f32 v175, v175\n\t"
          "v_exp_f32 v112, v112\n\t"
          "v_exp_f32 v113, v113\n\t"
          "v_mfma_f32_32x32x16_bf16 v[128:143], %[k0], %[qb16], v[128:143]\n\t"
          "s_waitcnt lgkmcnt(6)\n\t"
          "v_exp_f32 v114, v114\n\t"
          "v_exp_f32 v115, v115\n\t"
          "v_exp_f32 v116, v116\n\t"
          "v_mfma_f32_32x32x16_bf16 v[216:231], v[200:203], v[208:211], v[216:231]\n\t"
          "v_exp_f32 v117, v117\n\t"
          "v_exp_f32 v118, v118\n\t"
          "v_exp_f32 v119, v119\n\t"
          "v_smfmac_f32_32x32x32_bf16 %[ob], %[v01], v[160:167], %[idx]\n\t"
          "ds_read_b128 v[212:215], %[ub] offset:10736\n\t"
          "ds_read2_b64 v[188:191], %[fb] offset0:84 offset1:85\n\t"
          "v_exp_f32 v120, v120\n\t"
          "v_mfma_f32_32x32x16_bf16 v[232:247], v[204:207], v[208:211], v[232:247]\n\t"
          "v_exp_f32 v121, v121\n\t"
          "v_exp_f32 v122, v122\n\t"
          "v_exp_f32 v123, v123\n\t"
          "v_smfmac_f32_32x32x32_bf16 %[ob], %[v11], v[168:175], %[idx]\n\t"
          "s_waitcnt lgkmcnt(2)\n\t"
          "ds_read_b128 v[208:211], %[ub] offset:2032\n\t"
          "ds_read2_b64 v[200:203], %[fa] offset0:80 offset1:81\n\t"
          "v_exp_f32 v124, v124\n\t"
          "v_exp_f32 v125, v125\n\t"
          "v_mfma_f32_32x32x8_bf16 v[144:159], v[182:183], %[qa8e], 0\n\t"
          "v_exp_f32 v126, v126\n\t"
          "v_exp_f32 v127, v127\n\t"
          "v_exp_f32 v128, v128\n\t"
          "v_exp_f32 v129, v129\n\t"
          "v_mfma_f32_32x32x16_bf16 v[144:159], %[k1], %[qa16], v[144:159]\n\t"
          "v_exp_f32 v130, v130\n\t"
          "v_exp_f32 v131, v131\n\t"
          "v_exp_f32 v132, v132\n\t"
          "v_mfma_f32_32x32x16_bf16 v[232:247], v[184:187], v[196:199], v[232:247]\n\t"
          "v_exp_f32 v133, v133\n\t"
          "v_exp_f32 v134, v134\n\t"
          "v_exp_f32 v135, v135\n\t"
          "v_smfmac_f32_32x32x32_bf16 %[oa], %[v00], v[112:119], %[idx]\n\t"
          "ds_read2_b64 v[204:207], %[fb] offset0:80 offset1:81\n\t"
          "ds_read_b128 %[k0], %[ak] offset:18624\n\t"
          "v_exp_f32 v136, v136\n\t"
          "v_exp_f32 v137, v137\n\t"
          "v_mfma_f32_32x32x16_bf16 v[216:231], v[184:187], v[192:195], v[216:231]\n\t"
          "v_exp_f32 v138, v138\n\t"
          "v_exp_f32 v139, v139\n\t"
          "v_exp_f32 v140, v140\n\t"
          "v_smfmac_f32_32x32x32_bf16 %[oa], %[v10], v[120:127], %[idx]\n\t"
          "ds_read_b128 %[v01], %[av] offset:16576\n\t"
          "ds_read_b128 %[v11], %[av] offset:17248\n\t"
          "v_exp_f32 v141, v141\n\t"
          "v_exp_f32 v142, v142\n\t"
          "v_mfma_f32_32x32x8_bf16 v[160:175], v[182:183], %[qb8e], 0\n\t"
          "v_exp_f32 v143, v143\n\t"
          "v_exp_f32 v144, v144\n\t"
          "v_exp_f32 v145, v145\n\t"
          "v_exp_f32 v146, v146\n\t"
          "v_mfma_f32_32x32x16_bf16 v[160:175], %[k1], %[qb16], v[160:175]\n\t"
          "s_waitcnt lgkmcnt(6)\n\t"
          "v_add_u32 %[ak], %[dl], %[ak]\n\t"
          "v_add_u32 %[av], %[dl], %[av]\n\t"
          "v_exp_f32 v147, v147\n\t"
          "v_exp_f32 v148, v148\n\t"
          "v_mfma_f32_32x32x16_bf16 v[232:247], v[188:191], v[192:195], v[232:247]\n\t"
          "v_exp_f32 v149, v149\n\t"
          "v_exp_f32 v150, v150\n\t"
          "v_exp_f32 v151, v151\n\t"
          "v_smfmac_f32_32x32x32_bf16 %[ob], %[v00], v[128:135], %[idx]\n\t"
          "s_waitcnt lgkmcnt(4)\n\t"
          "ds_read2_b64 v[184:187], %[fa] offset0:92 offset1:93\n\t"
          "ds_read_b128 v[196:199], %[ub] offset:10336\n\t"
          "s_mul_i32 %[dl], %[dl], -1\n\t"
          "v_exp_f32 v152, v152\n\t"
          "v_mfma_f32_32x32x16_bf16 v[232:247], v[200:203], v[212:215], v[232:247]\n\t"
          "v_exp_f32 v153, v153\n\t"
          "v_exp_f32 v154, v154\n\t"
          "v_exp_f32 v155, v155\n\t"
          "v_smfmac_f32_32x32x32_bf16 %[ob], %[v10], v[136:143], %[idx]\n\t"
          "s_waitcnt lgkmcnt(2)\n\t"
          "ds_read2_b64 v[188:191], %[fb] offset0:92 offset1:93\n\t"
          "ds_read_b128 v[192:195], %[ub] offset:1632\n\t"
          "s_barrier\n\t"
          "v_exp_f32 v156, v156\n\t"
          "v_exp_f32 v157, v157\n\t"
          "v_mfma_f32_32x32x8_bf16 v[112:127], v[182:183], %[qa8o], 0\n\t"
          "v_exp_f32 v158, v158\n\t"
          "v_exp_f32 v159, v159\n\t"
          "v_exp_f32 v160, v160\n\t"
          "v_exp_f32 v161, v161\n\t"
          "v_mfma_f32_32x32x16_bf16 v[112:127], %[k0], %[qa16], v[112:127]\n\t"
          "v_exp_f32 v162, v162\n\t"
          "v_exp_f32 v163, v163\n\t"
          "v_exp_f32 v164, v164\n\t"
          "v_mfma_f32_32x32x16_bf16 v[216:231], v[200:203], v[208:211], v[216:231]\n\t"
          "v_exp_f32 v165, v165\n\t"
          "v_exp_f32 v166, v166\n\t"
          "v_exp_f32 v167, v167\n\t"
          "v_smfmac_f32_32x32x32_bf16 %[oa], %[v01], v[144:151], %[idx]\n\t"
          "ds_read_b128 %[k1], %[ak] offset:2048\n\t"
          "ds_read_b128 v[176:179], %[ak] offset:20992\n\t"
          "v_exp_f32 v168, v168\n\t"
          "v_mfma_f32_32x32x16_bf16 v[232:247], v[204:207], v[208:211], v[232:247]\n\t"
          "v_exp_f32 v169, v169\n\t"
          "v_exp_f32 v170, v170\n\t"
          "v_exp_f32 v171, v171\n\t"
          "v_smfmac_f32_32x32x32_bf16 %[oa], %[v11], v[152:159], %[idx]\n\t"
          "ds_read_b128 v[212:215], %[ub] offset:10464\n\t"
          "ds_read_b128 %[v00], %[av] offset:0\n\t"
          "ds_read_b128 %[v10], %[av] offset:672\n\t"
          "v_exp_f32 v172, v172\n\t"
          "v_exp_f32 v173, v173\n\t"
          "v_mfma_f32_32x32x8_bf16 v[128:143], v[182:183], %[qb8o], 0\n\t"
          "v_exp_f32 v174, v174\n\t"
          "v_exp_f32 v175, v175\n\t"
          "v_exp_f32 v112, v112\n\t"
          "v_exp_f32 v113, v113\n\t"
          "v_mfma_f32_32x32x16_bf16 v[128:143], %[k0], %[qb16], v[128:143]\n\t"
          "s_waitcnt lgkmcnt(7)\n\t"
          "v_exp_f32 v114, v114\n\t"
          "v_exp_f32 v115, v115\n\t"
          "v_exp_f32 v116, v116\n\t"
          "v_mfma_f32_32x32x16_bf16 v[232:247], v[184:187], v[196:199], v[232:247]\n\t"
          "v_exp_f32 v117, v117\n\t"
          "v_exp_f32 v118, v118\n\t"
          "v_exp_f32 v119, v119\n\t"
          "v_smfmac_f32_32x32x32_bf16 %[ob], %[v01], v[160:167], %[idx]\n\t"
          "s_waitcnt lgkmcnt(5)\n\t"
          "ds_read_b128 v[208:211], %[ub] offset:1760\n\t"
          "ds_read2_b64 v[200:203], %[fa] offset0:88 offset1:89\n\t"
          "v_exp_f32 v120, v120\n\t"
          "v_mfma_f32_32x32x16_bf16 v[216:231], v[184:187], v[192:195], v[216:231]\n\t"
          "v_exp_f32 v121, v121\n\t"
          "v_exp_f32 v122, v122\n\t"
          "v_exp_f32 v123, v123\n\t"
          "v_smfmac_f32_32x32x32_bf16 %[ob], %[v11], v[168:175], %[idx]\n\t"
          "s_waitcnt lgkmcnt(2)\n\t"
          "ds_read_b128 v[196:199], %[ub] offset:10064\n\t"
          "ds_read2_b64 v[204:207], %[fb] offset0:88 offset1:89\n\t"
          "v_exp_f32 v124, v124\n\t"
          "v_exp_f32 v125, v125\n\t"
          "v_mfma_f32_32x32x8_bf16 v[144:159], v[176:177], %[qa8e], 0\n\t"
          "v_exp_f32 v126, v126\n\t"
          "v_exp_f32 v127, v127\n\t"
          "v_exp_f32 v128, v128\n\t"
          "v_exp_f32 v129, v129\n\t"
          "v_mfma_f32_32x32x16_bf16 v[144:159], %[k1], %[qa16], v[144:159]\n\t"
          "v_exp_f32 v130, v130\n\t"
          "v_exp_f32 v131, v131\n\t"
          "v_exp_f32 v132, v132\n\t"
          "v_mfma_f32_32x32x16_bf16 v[232:247], v[188:191], v[192:195], v[232:247]\n\t"
          "v_exp_f32 v133, v133\n\t"
          "v_exp_f32 v134, v134\n\t"
          "v_exp_f32 v135, v135\n\t"
          "v_smfmac_f32_32x32x32_bf16 %[oa], %[v00], v[112:119], %[idx]\n\t"
          "s_waitcnt lgkmcnt(2)\n\t"
          "ds_read2_b64 v[184:187], %[fa] offset0:100 offset1:101\n\t"
          "ds_read_b128 %[k0], %[ak] offset:4416\n\t"
          "v_exp_f32 v136, v136\n\t"
          "v_mfma_f32_32x32x16_bf16 v[232:247], v[200:203], v[212:215], v[232:247]\n\t"
          "v_exp_f32 v137, v137\n\t"
          "v_exp_f32 v138, v138\n\t"
          "v_exp_f32 v139, v139\n\t"
          "v_smfmac_f32_32x32x32_bf16 %[oa], %[v10], v[120:127], %[idx]\n\t"
          "ds_read_b128 v[192:195], %[ub] offset:1360\n\t"
          "ds_read_b128 %[v01], %[av] offset:2368\n\t"
          "ds_read_b128 %[v11], %[av] offset:3040\n\t"
          "v_exp_f32 v140, v140\n\t"
          "v_exp_f32 v141, v141\n\t"
          "v_mfma_f32_32x32x8_bf16 v[160:175], v[176:177], %[qb8e], 0\n\t"
          "v_exp_f32 v142, v142\n\t"
          "v_exp_f32 v143, v143\n\t"
          "v_exp_f32 v144, v144\n\t"
          "v_exp_f32 v145, v145\n\t"
          "v_mfma_f32_32x32x16_bf16 v[160:175], %[k1], %[qb16], v[160:175]\n\t"
          "v_exp_f32 v146, v146\n\t"
          "v_exp_f32 v147, v147\n\t"
          "v_exp_f32 v148, v148\n\t"
          "v_mfma_f32_32x32x16_bf16 v[216:231], v[200:203], v[208:211], v[216:231]\n\t"
          "v_exp_f32 v149, v149\n\t"
          "v_exp_f32 v150, v150\n\t"
          "v_exp_f32 v151, v151\n\t"
          "v_smfmac_f32_32x32x32_bf16 %[ob], %[v00], v[128:135], %[idx]\n\t"
          "s_waitcnt lgkmcnt(5)\n\t"
          "ds_read_b128 v[212:215], %[ub] offset:10192\n\t"
          "ds_read2_b64 v[188:191], %[fb] offset0:100 offset1:101\n\t"
          "v_exp_f32 v152, v152\n\t"
          "v_mfma_f32_32x32x16_bf16 v[232:247], v[204:207], v[208:211], v[232:247]\n\t"
          "v_exp_f32 v153, v153\n\t"
          "v_exp_f32 v154, v154\n\t"
          "v_exp_f32 v155, v155\n\t"
          "v_smfmac_f32_32x32x32_bf16 %[ob], %[v10], v[136:143], %[idx]\n\t"
          "s_waitcnt lgkmcnt(2)\n\t"
          "ds_read_b128 v[208:211], %[ub] offset:1488\n\t"
          "ds_read2_b64 v[200:203], %[fa] offset0:96 offset1:97\n\t"
          "v_exp_f32 v156, v156\n\t"
          "v_exp_f32 v157, v157\n\t"
          "v_mfma_f32_32x32x8_bf16 v[112:127], v[176:177], %[qa8o], 0\n\t"
          "v_exp_f32 v158, v158\n\t"
          "v_exp_f32 v159, v159\n\t"
          "v_exp_f32 v160, v160\n\t"
          "v_exp_f32 v161, v161\n\t"
          "v_mfma_f32_32x32x16_bf16 v[112:127], %[k0], %[qa16], v[112:127]\n\t"
          "v_exp_f32 v162, v162\n\t"
          "v_exp_f32 v163, v163\n\t"
          "v_exp_f32 v164, v164\n\t"
          "v_mfma_f32_32x32x16_bf16 v[232:247], v[184:187], v[196:199], v[232:247]\n\t"
          "v_exp_f32 v165, v165\n\t"
          "v_exp_f32 v166, v166\n\t"
          "v_exp_f32 v167, v167\n\t"
          "v_smfmac_f32_32x32x32_bf16 %[oa], %[v01], v[144:151], %[idx]\n\t"
          "ds_read2_b64 v[204:207], %[fb] offset0:96 offset1:97\n\t"
          "ds_read_b128 %[k1], %[ak] offset:6784\n\t"
          "v_exp_f32 v168, v168\n\t"
          "v_mfma_f32_32x32x16_bf16 v[216:231], v[184:187], v[192:195], v[216:231]\n\t"
          "v_exp_f32 v169, v169\n\t"
          "v_exp_f32 v170, v170\n\t"
          "v_exp_f32 v171, v171\n\t"
          "v_smfmac_f32_32x32x32_bf16 %[oa], %[v11], v[152:159], %[idx]\n\t"
          "ds_read_b128 %[v00], %[av] offset:4736\n\t"
          "ds_read_b128 %[v10], %[av] offset:5408\n\t"
          "v_exp_f32 v172, v172\n\t"
          "v_exp_f32 v173, v173\n\t"
          "v_mfma_f32_32x32x8_bf16 v[128:143], v[176:177], %[qb8o], 0\n\t"
          "v_exp_f32 v174, v174\n\t"
          "v_exp_f32 v175, v175\n\t"
          "v_exp_f32 v112, v112\n\t"
          "v_exp_f32 v113, v113\n\t"
          "v_mfma_f32_32x32x16_bf16 v[128:143], %[k0], %[qb16], v[128:143]\n\t"
          "s_waitcnt lgkmcnt(6)\n\t"
          "v_exp_f32 v114, v114\n\t"
          "v_exp_f32 v115, v115\n\t"
          "v_exp_f32 v116, v116\n\t"
          "v_mfma_f32_32x32x16_bf16 v[232:247], v[188:191], v[192:195], v[232:247]\n\t"
          "v_exp_f32 v117, v117\n\t"
          "v_exp_f32 v118, v118\n\t"
          "v_exp_f32 v119, v119\n\t"
          "v_smfmac_f32_32x32x32_bf16 %[ob], %[v01], v[160:167], %[idx]\n\t"
          "s_waitcnt lgkmcnt(4)\n\t"
          "ds_read2_b64 v[184:187], %[fa] offset0:108 offset1:109\n\t"
          "ds_read_b128 v[196:199], %[ub] offset:9792\n\t"
          "v_exp_f32 v120, v120\n\t"
          "v_mfma_f32_32x32x16_bf16 v[232:247], v[200:203], v[212:215], v[232:247]\n\t"
          "v_exp_f32 v121, v121\n\t"
          "v_exp_f32 v122, v122\n\t"
          "v_exp_f32 v123, v123\n\t"
          "v_smfmac_f32_32x32x32_bf16 %[ob], %[v11], v[168:175], %[idx]\n\t"
          "s_waitcnt lgkmcnt(2)\n\t"
          "ds_read2_b64 v[188:191], %[fb] offset0:108 offset1:109\n\t"
          "ds_read_b128 v[192:195], %[ub] offset:1088\n\t"
          "v_exp_f32 v124, v124\n\t"
          "v_exp_f32 v125, v125\n\t"
          "v_mfma_f32_32x32x8_bf16 v[144:159], v[178:179], %[qa8e], 0\n\t"
          "v_exp_f32 v126, v126\n\t"
          "v_exp_f32 v127, v127\n\t"
          "v_exp_f32 v128, v128\n\t"
          "v_exp_f32 v129, v129\n\t"
          "v_mfma_f32_32x32x16_bf16 v[144:159], %[k1], %[qa16], v[144:159]\n\t"
          "v_exp_f32 v130, v130\n\t"
          "v_exp_f32 v131, v131\n\t"
          "v_exp_f32 v132, v132\n\t"
          "v_mfma_f32_32x32x16_bf16 v[216:231], v[200:203], v[208:211], v[216:231]\n\t"
          "v_exp_f32 v133, v133\n\t"
          "v_exp_f32 v134, v134\n\t"
          "v_exp_f32 v135, v135\n\t"
          "v_smfmac_f32_32x32x32_bf16 %[oa], %[v00], v[112:119], %[idx]\n\t"
          "ds_read_b128 v[212:215], %[ub] offset:9920\n\t"
          "ds_read_b128 %[k0], %[ak] offset:9152\n\t"
          "v_exp_f32 v136, v136\n\t"
          "v_mfma_f32_32x32x16_bf16 v[232:247], v[204:207], v[208:211], v[232:247]\n\t"
          "v_exp_f32 v137, v137\n\t"
          "v_exp_f32 v138, v138\n\t"
          "v_exp_f32 v139, v139\n\t"
          "v_smfmac_f32_32x32x32_bf16 %[oa], %[v10], v[120:127], %[idx]\n\t"
          "ds_read2_b64 v[200:203], %[fa] offset0:104 offset1:105\n\t"
          "ds_read_b128 %[v01], %[av] offset:7104\n\t"
          "ds_read_b128 %[v11], %[av] offset:7776\n\t"
          "v_exp_f32 v140, v140\n\t"
          "v_exp_f32 v141, v141\n\t"
          "v_mfma_f32_32x32x8_bf16 v[160:175], v[178:179], %[qb8e], 0\n\t"
          "v_exp_f32 v142, v142\n\t"
          "v_exp_f32 v143, v143\n\t"
          "v_exp_f32 v144, v144\n\t"
          "v_exp_f32 v145, v145\n\t"
          "v_mfma_f32_32x32x16_bf16 v[160:175], %[k1], %[qb16], v[160:175]\n\t"
          "s_waitcnt lgkmcnt(7)\n\t"
          "v_exp_f32 v146, v146\n\t"
          "v_exp_f32 v147, v147\n\t"
          "v_exp_f32 v148, v148\n\t"
          "v_mfma_f32_32x32x16_bf16 v[232:247], v[184:187], v[196:199], v[232:247]\n\t"
          "v_exp_f32 v149, v149\n\t"
          "v_exp_f32 v150, v150\n\t"
          "v_exp_f32 v151, v151\n\t"
          "v_smfmac_f32_32x32x32_bf16 %[ob], %[v00], v[128:135], %[idx]\n\t"
          "s_waitcnt lgkmcnt(5)\n\t"
          "ds_read2_b64 v[204:207], %[fb] offset0:104 offset1:105\n\t"
          "ds_read_b128 v[208:211], %[ub] offset:1216\n\t"
          "v_exp_f32 v152, v152\n\t"
          "v_mfma_f32_32x32x16_bf16 v[216:231], v[184:187], v[192:195], v[216:231]\n\t"
          "v_exp_f32 v153, v153\n\t"
          "v_exp_f32 v154, v154\n\t"
          "v_exp_f32 v155, v155\n\t"
          "v_smfmac_f32_32x32x32_bf16 %[ob], %[v10], v[136:143], %[idx]\n\t"
          "s_waitcnt lgkmcnt(2)\n\t"
          "ds_read2_b64 v[184:187], %[fa] offset0:116 offset1:117\n\t"
          "ds_read_b128 v[196:199], %[ub] offset:9520\n\t"
          "v_exp_f32 v156, v156\n\t"
          "v_exp_f32 v157, v157\n\t"
          "v_mfma_f32_32x32x8_bf16 v[112:127], v[178:179], %[qa8o], 0\n\t"
          "v_exp_f32 v158, v158\n\t"
          "v_exp_f32 v159, v159\n\t"
          "v_exp_f32 v160, v160\n\t"
          "v_exp_f32 v161, v161\n\t"
          "v_mfma_f32_32x32x16_bf16 v[112:127], %[k0], %[qa16], v[112:127]\n\t"
          "v_exp_f32 v162, v162\n\t"
          "v_exp_f32 v163, v163\n\t"
          "v_exp_f32 v164, v164\n\t"
          "v_mfma_f32_32x32x16_bf16 v[232:247], v[188:191], v[192:195], v[232:247]\n\t"
          "v_exp_f32 v165, v165\n\t"
          "v_exp_f32 v166, v166\n\t"
          "v_exp_f32 v167, v167\n\t"
          "v_smfmac_f32_32x32x32_bf16 %[oa], %[v01], v[144:151], %[idx]\n\t"
          "ds_read_b128 %[k1], %[ak] offset:11520\n\t"
          "ds_read_b128 v[180:183], %[ak] offset:22016\n\t"
          "v_exp_f32 v168, v168\n\t"
          "v_mfma_f32_32x32x16_bf16 v[232:247], v[200:203], v[212:215], v[232:247]\n\t"
          "v_exp_f32 v169, v169\n\t"
          "v_exp_f32 v170, v170\n\t"
          "v_exp_f32 v171, v171\n\t"
          "v_smfmac_f32_32x32x32_bf16 %[oa], %[v11], v[152:159], %[idx]\n\t"
          "ds_read_b128 v[192:195], %[ub] offset:816\n\t"
          "ds_read_b128 %[v00], %[av] offset:9472\n\t"
          "ds_read_b128 %[v10], %[av] offset:10144\n\t"
          "v_exp_f32 v172, v172\n\t"
          "v_exp_f32 v173, v173\n\t"
          "v_mfma_f32_32x32x8_bf16 v[128:143], v[178:179], %[qb8o], 0\n\t"
          "v_exp_f32 v174, v174\n\t"
          "v_exp_f32 v175, v175\n\t"
          "v_exp_f32 v112, v112\n\t"
          "v_exp_f32 v113, v113\n\t"
          "v_mfma_f32_32x32x16_bf16 v[128:143], %[k0], %[qb16], v[128:143]\n\t"
          "s_waitcnt lgkmcnt(7)\n\t"
          "v_exp_f32 v114, v114\n\t"
          "v_exp_f32 v115, v115\n\t"
          "v_exp_f32 v116, v116\n\t"
          "v_mfma_f32_32x32x16_bf16 v[216:231], v[200:203], v[208:211], v[216:231]\n\t"
          "v_exp_f32 v117, v117\n\t"
          "v_exp_f32 v118, v118\n\t"
          "v_exp_f32 v119, v119\n\t"
          "v_smfmac_f32_32x32x32_bf16 %[ob], %[v01], v[160:167], %[idx]\n\t"
          "ds_read_b128 v[212:215], %[ub] offset:9648\n\t"
          "ds_read2_b64 v[188:191], %[fb] offset0:116 offset1:117\n\t"
          "v_exp_f32 v120, v120\n\t"
          "v_mfma_f32_32x32x16_bf16 v[232:247], v[204:207], v[208:211], v[232:247]\n\t"
          "v_exp_f32 v121, v121\n\t"
          "v_exp_f32 v122, v122\n\t"
          "v_exp_f32 v123, v123\n\t"
          "v_smfmac_f32_32x32x32_bf16 %[ob], %[v11], v[168:175], %[idx]\n\t"
          "s_waitcnt lgkmcnt(2)\n\t"
          "ds_read_b128 v[208:211], %[ub] offset:944\n\t"
          "ds_read2_b64 v[200:203], %[fa] offset0:112 offset1:113\n\t"
          "v_exp_f32 v124, v124\n\t"
          "v_exp_f32 v125, v125\n\t"
          "v_mfma_f32_32x32x8_bf16 v[144:159], v[180:181], %[qa8e], 0\n\t"
          "v_exp_f32 v126, v126\n\t"
          "v_exp_f32 v127, v127\n\t"
          "v_exp_f32 v128, v128\n\t"
          "v_exp_f32 v129, v129\n\t"
          "v_mfma_f32_32x32x16_bf16 v[144:159], %[k1], %[qa16], v[144:159]\n\t"
          "v_exp_f32 v130, v130\n\t"
          "v_exp_f32 v131, v131\n\t"
          "v_exp_f32 v132, v132\n\t"
          "v_mfma_f32_32x32x16_bf16 v[232:247], v[184:187], v[196:199], v[232:247]\n\t"
          "v_exp_f32 v133, v133\n\t"
          "v_exp_f32 v134, v134\n\t"
          "v_exp_f32 v135, v135\n\t"
          "v_smfmac_f32_32x32x32_bf16 %[oa], %[v00], v[112:119], %[idx]\n\t"
          "ds_read2_b64 v[204:207], %[fb] offset0:112 offset1:113\n\t"
          "ds_read_b128 %[k0], %[ak] offset:13888\n\t"
          "v_exp_f32 v136, v136\n\t"
          "v_mfma_f32_32x32x16_bf16 v[216:231], v[184:187], v[192:195], v[216:231]\n\t"
          "v_exp_f32 v137, v137\n\t"
          "v_exp_f32 v138, v138\n\t"
          "v_exp_f32 v139, v139\n\t"
          "v_smfmac_f32_32x32x32_bf16 %[oa], %[v10], v[120:127], %[idx]\n\t"
          "ds_read_b128 %[v01], %[av] offset:11840\n\t"
          "ds_read_b128 %[v11], %[av] offset:12512\n\t"
          "v_exp_f32 v140, v140\n\t"
          "v_exp_f32 v141, v141\n\t"
          "v_mfma_f32_32x32x8_bf16 v[160:175], v[180:181], %[qb8e], 0\n\t"
          "v_exp_f32 v142, v142\n\t"
          "v_exp_f32 v143, v143\n\t"
          "v_exp_f32 v144, v144\n\t"
          "v_exp_f32 v145, v145\n\t"
          "v_mfma_f32_32x32x16_bf16 v[160:175], %[k1], %[qb16], v[160:175]\n\t"
          "s_waitcnt lgkmcnt(6)\n\t"
          "v_exp_f32 v146, v146\n\t"
          "v_exp_f32 v147, v147\n\t"
          "v_exp_f32 v148, v148\n\t"
          "v_mfma_f32_32x32x16_bf16 v[232:247], v[188:191], v[192:195], v[232:247]\n\t"
          "v_exp_f32 v149, v149\n\t"
          "v_exp_f32 v150, v150\n\t"
          "v_exp_f32 v151, v151\n\t"
          "v_smfmac_f32_32x32x32_bf16 %[ob], %[v00], v[128:135], %[idx]\n\t"
          "s_waitcnt lgkmcnt(4)\n\t"
          "ds_read2_b64 v[184:187], %[fa] offset0:124 offset1:125\n\t"
          "ds_read_b128 v[196:199], %[ub] offset:9248\n\t"
          "v_exp_f32 v152, v152\n\t"
          "v_mfma_f32_32x32x16_bf16 v[232:247], v[200:203], v[212:215], v[232:247]\n\t"
          "v_exp_f32 v153, v153\n\t"
          "v_exp_f32 v154, v154\n\t"
          "v_exp_f32 v155, v155\n\t"
          "v_smfmac_f32_32x32x32_bf16 %[ob], %[v10], v[136:143], %[idx]\n\t"
          "s_waitcnt lgkmcnt(2)\n\t"
          "ds_read2_b64 v[188:191], %[fb] offset0:124 offset1:125\n\t"
          "ds_read_b128 v[192:195], %[ub] offset:544\n\t"
          "v_exp_f32 v156, v156\n\t"
          "v_exp_f32 v157, v157\n\t"
          "v_mfma_f32_32x32x8_bf16 v[112:127], v[180:181], %[qa8o], 0\n\t"
          "v_exp_f32 v158, v158\n\t"
          "v_exp_f32 v159, v159\n\t"
          "v_exp_f32 v160, v160\n\t"
          "v_exp_f32 v161, v161\n\t"
          "v_mfma_f32_32x32x16_bf16 v[112:127], %[k0], %[qa16], v[112:127]\n\t"
          "v_exp_f32 v162, v162\n\t"
          "v_exp_f32 v163, v163\n\t"
          "v_exp_f32 v164, v164\n\t"
          "v_mfma_f32_32x32x16_bf16 v[216:231], v[200:203], v[208:211], v[216:231]\n\t"
          "v_exp_f32 v165, v165\n\t"
          "v_exp_f32 v166, v166\n\t"
          "v_exp_f32 v167, v167\n\t"
          "v_smfmac_f32_32x32x32_bf16 %[oa], %[v01], v[144:151], %[idx]\n\t"
          "ds_read_b128 v[212:215], %[ub] offset:9376\n\t"
          "ds_read_b128 %[k1], %[ak] offset:16256\n\t"
          "v_exp_f32 v168, v168\n\t"
          "v_exp_f32 v169, v169\n\t"
          "v_mfma_f32_32x32x16_bf16 v[232:247], v[204:207], v[208:211], v[232:247]\n\t"
          "v_exp_f32 v170, v170\n\t"
          "v_exp_f32 v171, v171\n\t"
          "v_exp_f32 v172, v172\n\t"
          "v_smfmac_f32_32x32x32_bf16 %[oa], %[v11], v[152:159], %[idx]\n\t"
          "ds_read2_b64 v[200:203], %[fa] offset0:120 offset1:121\n\t"
          "ds_read_b128 %[v00], %[av] offset:14208\n\t"
          "ds_read_b128 %[v10], %[av] offset:14880\n\t"
          "v_exp_f32 v173, v173\n\t"
          "v_exp_f32 v174, v174\n\t"
          "v_mfma_f32_32x32x8_bf16 v[128:143], v[180:181], %[qb8o], 0\n\t"
          "v_exp_f32 v175, v175\n\t"
          "v_exp_f32 v112, v112\n\t"
          "v_exp_f32 v113, v113\n\t"
          "v_exp_f32 v114, v114\n\t"
          "v_mfma_f32_32x32x16_bf16 v[128:143], %[k0], %[qb16], v[128:143]\n\t"
          "s_waitcnt lgkmcnt(7)\n\t"
          "s_sub_u32 %[ca], %[ca], 1\n\t"
          "v_exp_f32 v115, v115\n\t"
          "v_exp_f32 v116, v116\n\t"
          "v_exp_f32 v117, v117\n\t"
          "v_mfma_f32_32x32x16_bf16 v[232:247], v[184:187], v[196:199], v[232:247]\n\t"
          "v_exp_f32 v118, v118\n\t"
          "v_exp_f32 v119, v119\n\t"
          "v_exp_f32 v120, v120\n\t"
          "v_smfmac_f32_32x32x32_bf16 %[ob], %[v01], v[160:167], %[idx]\n\t"
          "s_waitcnt lgkmcnt(5)\n\t"
          "ds_read2_b64 v[204:207], %[fb] offset0:120 offset1:121\n\t"
          "ds_read_b128 v[208:211], %[ub] offset:672\n\t"
          "v_exp_f32 v121, v121\n\t"
          "v_exp_f32 v122, v122\n\t"
          "v_mfma_f32_32x32x16_bf16 v[216:231], v[184:187], v[192:195], v[216:231]\n\t"
          "v_exp_f32 v123, v123\n\t"
          "v_exp_f32 v124, v124\n\t"
          "v_exp_f32 v125, v125\n\t"
          "v_smfmac_f32_32x32x32_bf16 %[ob], %[v11], v[168:175], %[idx]\n\t"
          "s_waitcnt lgkmcnt(2)\n\t"
          "ds_read2_b64 v[184:187], %[fa] offset0:132 offset1:133\n\t"
          "ds_read_b128 v[196:199], %[ub] offset:8976\n\t"
          "v_exp_f32 v126, v126\n\t"
          "v_exp_f32 v127, v127\n\t"
          "v_mfma_f32_32x32x8_bf16 v[144:159], v[182:183], %[qa8e], 0\n\t"
          "v_exp_f32 v128, v128\n\t"
          "v_exp_f32 v129, v129\n\t"
          "v_exp_f32 v130, v130\n\t"
          "v_exp_f32 v131, v131\n\t"
          "v_mfma_f32_32x32x16_bf16 v[144:159], %[k1], %[qa16], v[144:159]\n\t"
          "s_cmp_lg_u32 %[ca], 0\n\t"
          "v_exp_f32 v132, v132\n\t"
          "v_exp_f32 v133, v133\n\t"
          "v_exp_f32 v134, v134\n\t"
          "v_mfma_f32_32x32x16_bf16 v[232:247], v[188:191], v[192:195], v[232:247]\n\t"
          "v_exp_f32 v135, v135\n\t"
          "v_exp_f32 v136, v136\n\t"
          "v_exp_f32 v137, v137\n\t"
          "v_smfmac_f32_32x32x32_bf16 %[oa], %[v00], v[112:119], %[idx]\n\t"
          "ds_read_b128 v[192:195], %[ub] offset:272\n\t"
          "ds_read_b128 %[k0], %[ak] offset:18624\n\t"
          "v_exp_f32 v138, v138\n\t"
          "v_exp_f32 v139, v139\n\t"
          "v_mfma_f32_32x32x16_bf16 v[232:247], v[200:203], v[212:215], v[232:247]\n\t"
          "v_exp_f32 v140, v140\n\t"
          "v_exp_f32 v141, v141\n\t"
          "v_exp_f32 v142, v142\n\t"
          "v_smfmac_f32_32x32x32_bf16 %[oa], %[v10], v[120:127], %[idx]\n\t"
          "ds_read_b128 %[v01], %[av] offset:16576\n\t"
          "ds_read_b128 %[v11], %[av] offset:17248\n\t"
          "v_exp_f32 v143, v143\n\t"
          "v_exp_f32 v144, v144\n\t"
          "v_mfma_f32_32x32x8_bf16 v[160:175], v[182:183], %[qb8e], 0\n\t"
          "v_exp_f32 v145, v145\n\t"
          "v_exp_f32 v146, v146\n\t"
          "v_exp_f32 v147, v147\n\t"
          "v_exp_f32 v148, v148\n\t"
          "v_mfma_f32_32x32x16_bf16 v[160:175], %[k1], %[qb16], v[160:175]\n\t"
          "s_waitcnt lgkmcnt(6)\n\t"
          "v_add_u32 %[ak], %[dl], %[ak]\n\t"
          "v_add_u32 %[av], %[dl], %[av]\n\t"
          "v_exp_f32 v149, v149\n\t"
          "v_exp_f32 v150, v150\n\t"
          "v_mfma_f32_32x32x16_bf16 v[216:231], v[200:203], v[208:211], v[216:231]\n\t"
          "v_exp_f32 v151, v151\n\t"
          "v_exp_f32 v152, v152\n\t"
          "v_exp_f32 v153, v153\n\t"
          "v_smfmac_f32_32x32x32_bf16 %[ob], %[v00], v[128:135], %[idx]\n\t"
          "ds_read_b128 v[212:215], %[ub] offset:9104\n\t"
          "ds_read2_b64 v[188:191], %[fb] offset0:132 offset1:133\n\t"
          "s_mul_i32 %[dl], %[dl], -1\n\t"
          "v_exp_f32 v154, v154\n\t"
          "v_mfma_f32_32x32x16_bf16 v[232:247], v[204:207], v[208:211], v[232:247]\n\t"
          "v_exp_f32 v155, v155\n\t"
          "v_exp_f32 v156, v156\n\t"
          "v_exp_f32 v157, v157\n\t"
          "v_smfmac_f32_32x32x32_bf16 %[ob], %[v10], v[136:143], %[idx]\n\t"
          "s_waitcnt lgkmcnt(2)\n\t"
          "ds_read_b128 v[208:211], %[ub] offset:400\n\t"
          "ds_read2_b64 v[200:203], %[fa] offset0:128 offset1:129\n\t"
          "s_barrier\n\t"
          "v_exp_f32 v158, v158\n\t"
          "v_exp_f32 v159, v159\n\t"
          "v_add_u32 %[fa], 0x400, %[fa]\n\t"
          "v_add_u32 %[fb], 0x400, %[fb]\n\t"
          "v_add_u32 %[ub], 0xffffef00, %[ub]\n\t"
          "s_cbranch_scc1 1b\n\t"
          "v_mfma_f32_32x32x8_bf16 v[112:127], v[182:183], %[qa8o], 0\n\t"
          "v_exp_f32 v160, v160\n\t"
          "v_exp_f32 v161, v161\n\t"
          "v_exp_f32 v162, v162\n\t"
          "v_exp_f32 v163, v163\n\t"
          "v_mfma_f32_32x32x16_bf16 v[112:127], %[k0], %[qa16], v[112:127]\n\t"
          "v_exp_f32 v164, v164\n\t"
          "v_exp_f32 v165, v165\n\t"
          "v_exp_f32 v166, v166\n\t"
          "v_exp_f32 v167, v167\n\t"
          "v_smfmac_f32_32x32x32_bf16 %[oa], %[v01], v[144:151], %[idx]\n\t"
          "ds_read_b128 %[k1], %[ak] offset:2048\n\t"
          "ds_read_b128 v[176:179], %[ak] offset:20992\n\t"
          "v_exp_f32 v168, v168\n\t"
          "v_exp_f32 v169, v169\n\t"
          "v_exp_f32 v170, v170\n\t"
          "v_exp_f32 v171, v171\n\t"
          "v_smfmac_f32_32x32x32_bf16 %[oa], %[v11], v[152:159], %[idx]\n\t"
          "ds_read_b128 %[v00], %[av] offset:0\n\t"
          "ds_read_b128 %[v10], %[av] offset:672\n\t"
          "v_exp_f32 v172, v172\n\t"
          "v_exp_f32 v173, v173\n\t"
          "v_exp_f32 v174, v174\n\t"
          "v_exp_f32 v175, v175\n\t"
          "v_mfma_f32_32x32x8_bf16 v[128:143], v[182:183], %[qb8o], 0\n\t"
          "v_exp_f32 v112, v112\n\t"
          "v_exp_f32 v113, v113\n\t"
          "v_exp_f32 v114, v114\n\t"
          "v_exp_f32 v115, v115\n\t"
          "v_mfma_f32_32x32x16_bf16 v[128:143], %[k0], %[qb16], v[128:143]\n\t"
          "v_exp_f32 v116, v116\n\t"
          "v_exp_f32 v117, v117\n\t"
          "v_exp_f32 v118, v118\n\t"
          "v_exp_f32 v119, v119\n\t"
          "v_smfmac_f32_32x32x32_bf16 %[ob], %[v01], v[160:167], %[idx]\n\t"
          "v_exp_f32 v120, v120\n\t"
          "v_exp_f32 v121, v121\n\t"
          "v_exp_f32 v122, v122\n\t"
          "v_exp_f32 v123, v123\n\t"
          "v_smfmac_f32_32x32x32_bf16 %[ob], %[v11], v[168:175], %[idx]\n\t"
          "s_waitcnt lgkmcnt(0)\n\t"
          "v_exp_f32 v124, v124\n\t"
          "v_exp_f32 v125, v125\n\t"
          "v_exp_f32 v126, v126\n\t"
          "v_exp_f32 v127, v127\n\t"
          "v_mfma_f32_32x32x8_bf16 v[144:159], v[176:177], %[qa8e], 0\n\t"
          "v_exp_f32 v128, v128\n\t"
          "v_exp_f32 v129, v129\n\t"
          "v_exp_f32 v130, v130\n\t"
          "v_exp_f32 v131, v131\n\t"
          "v_mfma_f32_32x32x16_bf16 v[144:159], %[k1], %[qa16], v[144:159]\n\t"
          "v_mbcnt_lo_u32_b32 v200, -1, 0\n\t"
          "v_mbcnt_hi_u32_b32 v200, -1, v200\n\t"
          "v_lshrrev_b32 v201, 5, v200\n\t"
          "v_and_b32 v200, 31, v200\n\t"
          "v_lshlrev_b32 v201, 3, v201\n\t"
          "v_lshl_add_u32 v200, v200, 6, v201\n\t"
          "v_add_u32 v200, %[yd], v200\n\t"
          "v_mov_b32 v201, %[yf]\n\t"
          "v_cvt_pk_bf16_f32 v184, v216, v217\n\t"
          "v_cvt_pk_bf16_f32 v185, v218, v219\n\t"
          "v_cvt_pk_bf16_f32 v186, v220, v221\n\t"
          "v_cvt_pk_bf16_f32 v187, v222, v223\n\t"
          "v_exp_f32 v132, v132\n\t"
          "v_exp_f32 v133, v133\n\t"
          "v_exp_f32 v134, v134\n\t"
          "v_exp_f32 v135, v135\n\t"
          "v_smfmac_f32_32x32x32_bf16 %[oa], %[v00], v[112:119], %[idx]\n\t"
          "ds_read_b128 %[k0], %[ak] offset:4416\n\t"
          "v_exp_f32 v136, v136\n\t"
          "v_exp_f32 v137, v137\n\t"
          "v_exp_f32 v138, v138\n\t"
          "v_exp_f32 v139, v139\n\t"
          "v_smfmac_f32_32x32x32_bf16 %[oa], %[v10], v[120:127], %[idx]\n\t"
          "ds_read_b128 %[v01], %[av] offset:2368\n\t"
          "ds_read_b128 %[v11], %[av] offset:3040\n\t"
          "ds_write_b64 v200, v[184:185] offset:0\n\t"
          "ds_write_b64 v200, v[186:187] offset:16\n\t"
          "v_exp_f32 v140, v140\n\t"
          "v_exp_f32 v141, v141\n\t"
          "v_exp_f32 v142, v142\n\t"
          "v_exp_f32 v143, v143\n\t"
          "v_mfma_f32_32x32x8_bf16 v[160:175], v[176:177], %[qb8e], 0\n\t"
          "v_exp_f32 v144, v144\n\t"
          "v_exp_f32 v145, v145\n\t"
          "v_exp_f32 v146, v146\n\t"
          "v_exp_f32 v147, v147\n\t"
          "v_mfma_f32_32x32x16_bf16 v[160:175], %[k1], %[qb16], v[160:175]\n\t"
          "v_cvt_pk_bf16_f32 v188, v224, v225\n\t"
          "v_cvt_pk_bf16_f32 v189, v226, v227\n\t"
          "v_cvt_pk_bf16_f32 v190, v228, v229\n\t"
          "v_cvt_pk_bf16_f32 v191, v230, v231\n\t"
          "v_exp_f32 v148, v148\n\t"
          "v_exp_f32 v149, v149\n\t"
          "v_exp_f32 v150, v150\n\t"
          "v_exp_f32 v151, v151\n\t"
          "v_smfmac_f32_32x32x32_bf16 %[ob], %[v00], v[128:135], %[idx]\n\t"
          "ds_write_b64 v200, v[188:189] offset:32\n\t"
          "ds_write_b64 v200, v[190:191] offset:48\n\t"
          "v_exp_f32 v152, v152\n\t"
          "v_exp_f32 v153, v153\n\t"
          "v_exp_f32 v154, v154\n\t"
          "v_exp_f32 v155, v155\n\t"
          "v_smfmac_f32_32x32x32_bf16 %[ob], %[v10], v[136:143], %[idx]\n\t"
          "s_waitcnt lgkmcnt(2)\n\t"
          "v_exp_f32 v156, v156\n\t"
          "v_exp_f32 v157, v157\n\t"
          "v_exp_f32 v158, v158\n\t"
          "v_exp_f32 v159, v159\n\t"
          "v_mfma_f32_32x32x8_bf16 v[112:127], v[176:177], %[qa8o], 0\n\t"
          "v_exp_f32 v160, v160\n\t"
          "v_exp_f32 v161, v161\n\t"
          "v_exp_f32 v162, v162\n\t"
          "v_exp_f32 v163, v163\n\t"
          "v_mfma_f32_32x32x16_bf16 v[112:127], %[k0], %[qa16], v[112:127]\n\t"
          "v_cvt_pk_bf16_f32 v192, v232, v233\n\t"
          "v_cvt_pk_bf16_f32 v193, v234, v235\n\t"
          "v_cvt_pk_bf16_f32 v194, v236, v237\n\t"
          "v_cvt_pk_bf16_f32 v195, v238, v239\n\t"
          "v_exp_f32 v164, v164\n\t"
          "v_exp_f32 v165, v165\n\t"
          "v_exp_f32 v166, v166\n\t"
          "v_exp_f32 v167, v167\n\t"
          "v_smfmac_f32_32x32x32_bf16 %[oa], %[v01], v[144:151], %[idx]\n\t"
          "ds_read_b128 %[k1], %[ak] offset:6784\n\t"
          "v_exp_f32 v168, v168\n\t"
          "v_exp_f32 v169, v169\n\t"
          "v_exp_f32 v170, v170\n\t"
          "v_exp_f32 v171, v171\n\t"
          "v_smfmac_f32_32x32x32_bf16 %[oa], %[v11], v[152:159], %[idx]\n\t"
          "ds_read_b128 %[v00], %[av] offset:4736\n\t"
          "ds_read_b128 %[v10], %[av] offset:5408\n\t"
          "ds_write_b64 v200, v[192:193] offset:2048\n\t"
          "ds_write_b64 v200, v[194:195] offset:2064\n\t"
          "v_exp_f32 v172, v172\n\t"
          "v_exp_f32 v173, v173\n\t"
          "v_exp_f32 v174, v174\n\t"
          "v_exp_f32 v175, v175\n\t"
          "v_mfma_f32_32x32x8_bf16 v[128:143], v[176:177], %[qb8o], 0\n\t"
          "v_exp_f32 v112, v112\n\t"
          "v_exp_f32 v113, v113\n\t"
          "v_exp_f32 v114, v114\n\t"
          "v_exp_f32 v115, v115\n\t"
          "v_mfma_f32_32x32x16_bf16 v[128:143], %[k0], %[qb16], v[128:143]\n\t"
          "v_cvt_pk_bf16_f32 v196, v240, v241\n\t"
          "v_cvt_pk_bf16_f32 v197, v242, v243\n\t"
          "v_cvt_pk_bf16_f32 v198, v244, v245\n\t"
          "v_cvt_pk_bf16_f32 v199, v246, v247\n\t"
          "v_exp_f32 v116, v116\n\t"
          "v_exp_f32 v117, v117\n\t"
          "v_exp_f32 v118, v118\n\t"
          "v_exp_f32 v119, v119\n\t"
          "v_smfmac_f32_32x32x32_bf16 %[ob], %[v01], v[160:167], %[idx]\n\t"
          "ds_write_b64 v200, v[196:197] offset:2080\n\t"
          "ds_write_b64 v200, v[198:199] offset:2096\n\t"
          "ds_write_b32 v201, v201\n\t"
          "v_exp_f32 v120, v120\n\t"
          "v_exp_f32 v121, v121\n\t"
          "v_exp_f32 v122, v122\n\t"
          "v_exp_f32 v123, v123\n\t"
          "v_smfmac_f32_32x32x32_bf16 %[ob], %[v11], v[168:175], %[idx]\n\t"
          "s_waitcnt lgkmcnt(3)\n\t"
          "v_exp_f32 v124, v124\n\t"
          "v_exp_f32 v125, v125\n\t"
          "v_exp_f32 v126, v126\n\t"
          "v_exp_f32 v127, v127\n\t"
          "v_mfma_f32_32x32x8_bf16 v[144:159], v[178:179], %[qa8e], 0\n\t"
          "v_exp_f32 v128, v128\n\t"
          "v_exp_f32 v129, v129\n\t"
          "v_exp_f32 v130, v130\n\t"
          "v_exp_f32 v131, v131\n\t"
          "v_mfma_f32_32x32x16_bf16 v[144:159], %[k1], %[qa16], v[144:159]\n\t"
          "v_exp_f32 v132, v132\n\t"
          "v_exp_f32 v133, v133\n\t"
          "v_exp_f32 v134, v134\n\t"
          "v_exp_f32 v135, v135\n\t"
          "v_smfmac_f32_32x32x32_bf16 %[oa], %[v00], v[112:119], %[idx]\n\t"
          "ds_read_b128 %[k0], %[ak] offset:9152\n\t"
          "v_exp_f32 v136, v136\n\t"
          "v_exp_f32 v137, v137\n\t"
          "v_exp_f32 v138, v138\n\t"
          "v_exp_f32 v139, v139\n\t"
          "v_smfmac_f32_32x32x32_bf16 %[oa], %[v10], v[120:127], %[idx]\n\t"
          "ds_read_b128 %[v01], %[av] offset:7104\n\t"
          "ds_read_b128 %[v11], %[av] offset:7776\n\t"
          "v_exp_f32 v140, v140\n\t"
          "v_exp_f32 v141, v141\n\t"
          "v_exp_f32 v142, v142\n\t"
          "v_exp_f32 v143, v143\n\t"
          "v_mfma_f32_32x32x8_bf16 v[160:175], v[178:179], %[qb8e], 0\n\t"
          "v_exp_f32 v144, v144\n\t"
          "v_exp_f32 v145, v145\n\t"
          "v_exp_f32 v146, v146\n\t"
          "v_exp_f32 v147, v147\n\t"
          "v_mfma_f32_32x32x16_bf16 v[160:175], %[k1], %[qb16], v[160:175]\n\t"
          "v_exp_f32 v148, v148\n\t"
          "v_exp_f32 v149, v149\n\t"
          "v_exp_f32 v150, v150\n\t"
          "v_exp_f32 v151, v151\n\t"
          "v_smfmac_f32_32x32x32_bf16 %[ob], %[v00], v[128:135], %[idx]\n\t"
          "v_exp_f32 v152, v152\n\t"
          "v_exp_f32 v153, v153\n\t"
          "v_exp_f32 v154, v154\n\t"
          "v_exp_f32 v155, v155\n\t"
          "v_smfmac_f32_32x32x32_bf16 %[ob], %[v10], v[136:143], %[idx]\n\t"
          "s_waitcnt lgkmcnt(0)\n\t"
          "v_exp_f32 v156, v156\n\t"
          "v_exp_f32 v157, v157\n\t"
          "v_exp_f32 v158, v158\n\t"
          "v_exp_f32 v159, v159\n\t"
          "v_mfma_f32_32x32x8_bf16 v[112:127], v[178:179], %[qa8o], 0\n\t"
          "v_exp_f32 v160, v160\n\t"
          "v_exp_f32 v161, v161\n\t"
          "v_exp_f32 v162, v162\n\t"
          "v_exp_f32 v163, v163\n\t"
          "v_mfma_f32_32x32x16_bf16 v[112:127], %[k0], %[qa16], v[112:127]\n\t"
          "v_exp_f32 v164, v164\n\t"
          "v_exp_f32 v165, v165\n\t"
          "v_exp_f32 v166, v166\n\t"
          "v_exp_f32 v167, v167\n\t"
          "v_smfmac_f32_32x32x32_bf16 %[oa], %[v01], v[144:151], %[idx]\n\t"
          "ds_read_b128 %[k1], %[ak] offset:11520\n\t"
          "ds_read_b128 v[180:183], %[ak] offset:22016\n\t"
          "v_exp_f32 v168, v168\n\t"
          "v_exp_f32 v169, v169\n\t"
          "v_exp_f32 v170, v170\n\t"
          "v_exp_f32 v171, v171\n\t"
          "v_smfmac_f32_32x32x32_bf16 %[oa], %[v11], v[152:159], %[idx]\n\t"
          "ds_read_b128 %[v00], %[av] offset:9472\n\t"
          "ds_read_b128 %[v10], %[av] offset:10144\n\t"
          "v_exp_f32 v172, v172\n\t"
          "v_exp_f32 v173, v173\n\t"
          "v_exp_f32 v174, v174\n\t"
          "v_exp_f32 v175, v175\n\t"
          "v_mfma_f32_32x32x8_bf16 v[128:143], v[178:179], %[qb8o], 0\n\t"
          "v_exp_f32 v112, v112\n\t"
          "v_exp_f32 v113, v113\n\t"
          "v_exp_f32 v114, v114\n\t"
          "v_exp_f32 v115, v115\n\t"
          "v_mfma_f32_32x32x16_bf16 v[128:143], %[k0], %[qb16], v[128:143]\n\t"
          "v_exp_f32 v116, v116\n\t"
          "v_exp_f32 v117, v117\n\t"
          "v_exp_f32 v118, v118\n\t"
          "v_exp_f32 v119, v119\n\t"
          "v_smfmac_f32_32x32x32_bf16 %[ob], %[v01], v[160:167], %[idx]\n\t"
          "v_exp_f32 v120, v120\n\t"
          "v_exp_f32 v121, v121\n\t"
          "v_exp_f32 v122, v122\n\t"
          "v_exp_f32 v123, v123\n\t"
          "v_smfmac_f32_32x32x32_bf16 %[ob], %[v11], v[168:175], %[idx]\n\t"
          "s_waitcnt lgkmcnt(0)\n\t"
          "v_exp_f32 v124, v124\n\t"
          "v_exp_f32 v125, v125\n\t"
          "v_exp_f32 v126, v126\n\t"
          "v_exp_f32 v127, v127\n\t"
          "v_mfma_f32_32x32x8_bf16 v[144:159], v[180:181], %[qa8e], 0\n\t"
          "v_exp_f32 v128, v128\n\t"
          "v_exp_f32 v129, v129\n\t"
          "v_exp_f32 v130, v130\n\t"
          "v_exp_f32 v131, v131\n\t"
          "v_mfma_f32_32x32x16_bf16 v[144:159], %[k1], %[qa16], v[144:159]\n\t"
          "v_exp_f32 v132, v132\n\t"
          "v_exp_f32 v133, v133\n\t"
          "v_exp_f32 v134, v134\n\t"
          "v_exp_f32 v135, v135\n\t"
          "v_smfmac_f32_32x32x32_bf16 %[oa], %[v00], v[112:119], %[idx]\n\t"
          "ds_read_b128 %[k0], %[ak] offset:13888\n\t"
          "v_exp_f32 v136, v136\n\t"
          "v_exp_f32 v137, v137\n\t"
          "v_exp_f32 v138, v138\n\t"
          "v_exp_f32 v139, v139\n\t"
          "v_smfmac_f32_32x32x32_bf16 %[oa], %[v10], v[120:127], %[idx]\n\t"
          "ds_read_b128 %[v01], %[av] offset:11840\n\t"
          "ds_read_b128 %[v11], %[av] offset:12512\n\t"
          "v_exp_f32 v140, v140\n\t"
          "v_exp_f32 v141, v141\n\t"
          "v_exp_f32 v142, v142\n\t"
          "v_exp_f32 v143, v143\n\t"
          "v_mfma_f32_32x32x8_bf16 v[160:175], v[180:181], %[qb8e], 0\n\t"
          "v_exp_f32 v144, v144\n\t"
          "v_exp_f32 v145, v145\n\t"
          "v_exp_f32 v146, v146\n\t"
          "v_exp_f32 v147, v147\n\t"
          "v_mfma_f32_32x32x16_bf16 v[160:175], %[k1], %[qb16], v[160:175]\n\t"
          "v_exp_f32 v148, v148\n\t"
          "v_exp_f32 v149, v149\n\t"
          "v_exp_f32 v150, v150\n\t"
          "v_exp_f32 v151, v151\n\t"
          "v_smfmac_f32_32x32x32_bf16 %[ob], %[v00], v[128:135], %[idx]\n\t"
          "v_exp_f32 v152, v152\n\t"
          "v_exp_f32 v153, v153\n\t"
          "v_exp_f32 v154, v154\n\t"
          "v_exp_f32 v155, v155\n\t"
          "v_smfmac_f32_32x32x32_bf16 %[ob], %[v10], v[136:143], %[idx]\n\t"
          "s_waitcnt lgkmcnt(0)\n\t"
          "v_exp_f32 v156, v156\n\t"
          "v_exp_f32 v157, v157\n\t"
          "v_exp_f32 v158, v158\n\t"
          "v_exp_f32 v159, v159\n\t"
          "v_mfma_f32_32x32x8_bf16 v[112:127], v[180:181], %[qa8o], 0\n\t"
          "v_exp_f32 v160, v160\n\t"
          "v_exp_f32 v161, v161\n\t"
          "v_exp_f32 v162, v162\n\t"
          "v_exp_f32 v163, v163\n\t"
          "v_mfma_f32_32x32x16_bf16 v[112:127], %[k0], %[qa16], v[112:127]\n\t"
          "v_exp_f32 v164, v164\n\t"
          "v_exp_f32 v165, v165\n\t"
          "v_exp_f32 v166, v166\n\t"
          "v_exp_f32 v167, v167\n\t"
          "v_smfmac_f32_32x32x32_bf16 %[oa], %[v01], v[144:151], %[idx]\n\t"
          "ds_read_b128 %[k1], %[ak] offset:16256\n\t"
          "v_exp_f32 v168, v168\n\t"
          "v_exp_f32 v169, v169\n\t"
          "v_exp_f32 v170, v170\n\t"
          "v_exp_f32 v171, v171\n\t"
          "v_smfmac_f32_32x32x32_bf16 %[oa], %[v11], v[152:159], %[idx]\n\t"
          "ds_read_b128 %[v00], %[av] offset:14208\n\t"
          "ds_read_b128 %[v10], %[av] offset:14880\n\t"
          "v_exp_f32 v172, v172\n\t"
          "v_exp_f32 v173, v173\n\t"
          "v_exp_f32 v174, v174\n\t"
          "v_exp_f32 v175, v175\n\t"
          "v_mfma_f32_32x32x8_bf16 v[128:143], v[180:181], %[qb8o], 0\n\t"
          "v_exp_f32 v112, v112\n\t"
          "v_exp_f32 v113, v113\n\t"
          "v_exp_f32 v114, v114\n\t"
          "v_exp_f32 v115, v115\n\t"
          "v_mfma_f32_32x32x16_bf16 v[128:143], %[k0], %[qb16], v[128:143]\n\t"
          "v_exp_f32 v116, v116\n\t"
          "v_exp_f32 v117, v117\n\t"
          "v_exp_f32 v118, v118\n\t"
          "v_exp_f32 v119, v119\n\t"
          "v_smfmac_f32_32x32x32_bf16 %[ob], %[v01], v[160:167], %[idx]\n\t"
          "v_exp_f32 v120, v120\n\t"
          "v_exp_f32 v121, v121\n\t"
          "v_exp_f32 v122, v122\n\t"
          "v_exp_f32 v123, v123\n\t"
          "v_smfmac_f32_32x32x32_bf16 %[ob], %[v11], v[168:175], %[idx]\n\t"
          "s_waitcnt lgkmcnt(0)\n\t"
          "v_exp_f32 v124, v124\n\t"
          "v_exp_f32 v125, v125\n\t"
          "v_exp_f32 v126, v126\n\t"
          "v_exp_f32 v127, v127\n\t"
          "v_mfma_f32_32x32x8_bf16 v[144:159], v[182:183], %[qa8e], 0\n\t"
          "v_exp_f32 v128, v128\n\t"
          "v_exp_f32 v129, v129\n\t"
          "v_exp_f32 v130, v130\n\t"
          "v_exp_f32 v131, v131\n\t"
          "v_mfma_f32_32x32x16_bf16 v[144:159], %[k1], %[qa16], v[144:159]\n\t"
          "v_exp_f32 v132, v132\n\t"
          "v_exp_f32 v133, v133\n\t"
          "v_exp_f32 v134, v134\n\t"
          "v_exp_f32 v135, v135\n\t"
          "v_smfmac_f32_32x32x32_bf16 %[oa], %[v00], v[112:119], %[idx]\n\t"
          "ds_read_b128 %[k0], %[ak] offset:18624\n\t"
          "v_exp_f32 v136, v136\n\t"
          "v_exp_f32 v137, v137\n\t"
          "v_exp_f32 v138, v138\n\t"
          "v_exp_f32 v139, v139\n\t"
          "v_smfmac_f32_32x32x32_bf16 %[oa], %[v10], v[120:127], %[idx]\n\t"
          "ds_read_b128 %[v01], %[av] offset:16576\n\t"
          "ds_read_b128 %[v11], %[av] offset:17248\n\t"
          "v_exp_f32 v140, v140\n\t"
          "v_exp_f32 v141, v141\n\t"
          "v_exp_f32 v142, v142\n\t"
          "v_exp_f32 v143, v143\n\t"
          "v_mfma_f32_32x32x8_bf16 v[160:175], v[182:183], %[qb8e], 0\n\t"
          "v_exp_f32 v144, v144\n\t"
          "v_exp_f32 v145, v145\n\t"
          "v_exp_f32 v146, v146\n\t"
          "v_exp_f32 v147, v147\n\t"
          "v_mfma_f32_32x32x16_bf16 v[160:175], %[k1], %[qb16], v[160:175]\n\t"
          "v_add_u32 %[ak], %[dl], %[ak]\n\t"
          "v_add_u32 %[av], %[dl], %[av]\n\t"
          "v_exp_f32 v148, v148\n\t"
          "v_exp_f32 v149, v149\n\t"
          "v_exp_f32 v150, v150\n\t"
          "v_exp_f32 v151, v151\n\t"
          "v_smfmac_f32_32x32x32_bf16 %[ob], %[v00], v[128:135], %[idx]\n\t"
          "s_mul_i32 %[dl], %[dl], -1\n\t"
          "v_exp_f32 v152, v152\n\t"
          "v_exp_f32 v153, v153\n\t"
          "v_exp_f32 v154, v154\n\t"
          "v_exp_f32 v155, v155\n\t"
          "v_smfmac_f32_32x32x32_bf16 %[ob], %[v10], v[136:143], %[idx]\n\t"
          "s_waitcnt lgkmcnt(0)\n\t"
          "s_barrier\n\t"
          "v_exp_f32 v156, v156\n\t"
          "v_exp_f32 v157, v157\n\t"
          "v_exp_f32 v158, v158\n\t"
          "v_exp_f32 v159, v159\n\t"
          "s_branch 3f\n\t"
          "5:\n\t"
          "ds_read_b128 %[k0], %[ak]\n\t"
          "ds_read_b128 v[180:183], %[ak] offset:1024\n\t"
          "v_mov_b32 v160, 0\n\t"
          "v_mov_b32 v161, 0\n\t"
          "v_mov_b32 v162, 0\n\t"
          "v_mov_b32 v163, 0\n\t"
          "v_mov_b32 v164, 0\n\t"
          "v_mov_b32 v165, 0\n\t"
          "v_mov_b32 v166, 0\n\t"
          "v_mov_b32 v167, 0\n\t"
          "v_mov_b32 v168, 0\n\t"
          "v_mov_b32 v169, 0\n\t"
          "v_mov_b32 v170, 0\n\t"
          "v_mov_b32 v171, 0\n\t"
          "v_mov_b32 v172, 0\n\t"
          "v_mov_b32 v173, 0\n\t"
          "v_mov_b32 v174, 0\n\t"
          "v_mov_b32 v175, 0\n\t"
          "s_waitcnt lgkmcnt(0)\n\t"
          "v_mfma_f32_32x32x8_bf16 v[112:127], v[182:183], %[qa8o], 0\n\t"
          "v_mfma_f32_32x32x16_bf16 v[112:127], %[k0], %[qa16], v[112:127]\n\t"
          "ds_read_b128 %[k1], %[ak] offset:2048\n\t"
          "ds_read_b128 v[176:179], %[ak] offset:20992\n\t"
          "ds_read_b128 %[v00], %[av] offset:0\n\t"
          "ds_read_b128 %[v10], %[av] offset:672\n\t"
          "s_nop 7\n\t"
          "s_nop 3\n\t"
          "s_branch 4f\n\t"
          "3:\n\t"
          "v_mfma_f32_32x32x8_bf16 v[112:127], v[182:183], %[qa8o], 0\n\t"
          "v_exp_f32 v160, v160\n\t"
          "v_exp_f32 v161, v161\n\t"
          "v_exp_f32 v162, v162\n\t"
          "v_exp_f32 v163, v163\n\t"
          "v_mfma_f32_32x32x16_bf16 v[112:127], %[k0], %[qa16], v[112:127]\n\t"
          "v_exp_f32 v164, v164\n\t"
          "v_exp_f32 v165, v165\n\t"
          "v_exp_f32 v166, v166\n\t"
          "v_exp_f32 v167, v167\n\t"
          "v_smfmac_f32_32x32x32_bf16 %[oa], %[v01], v[144:151], %[idx]\n\t"
          "ds_read_b128 %[k1], %[ak] offset:2048\n\t"
          "ds_read_b128 v[176:179], %[ak] offset:20992\n\t"
          "v_exp_f32 v168, v168\n\t"
          "v_exp_f32 v169, v169\n\t"
          "v_exp_f32 v170, v170\n\t"
          "v_exp_f32 v171, v171\n\t"
          "v_smfmac_f32_32x32x32_bf16 %[oa], %[v11], v[152:159], %[idx]\n\t"
          "ds_read_b128 %[v00], %[av] offset:0\n\t"
          "ds_read_b128 %[v10], %[av] offset:672\n\t"
          "v_exp_f32 v172, v172\n\t"
          "v_exp_f32 v173, v173\n\t"
          "v_exp_f32 v174, v174\n\t"
          "v_exp_f32 v175, v175\n\t"
          "4:\n\t"
          "v_mfma_f32_32x32x8_bf16 v[128:143], v[182:183], %[qb8o], 0\n\t"
          "v_exp_f32 v112, v112\n\t"
          "v_exp_f32 v113, v113\n\t"
          "v_exp_f32 v114, v114\n\t"
          "v_exp_f32 v115, v115\n\t"
          "v_mfma_f32_32x32x16_bf16 v[128:143], %[k0], %[qb16], v[128:143]\n\t"
          "v_exp_f32 v116, v116\n\t"
          "v_exp_f32 v117, v117\n\t"
          "v_exp_f32 v118, v118\n\t"
          "v_exp_f32 v119, v119\n\t"
          "v_smfmac_f32_32x32x32_bf16 %[ob], %[v01], v[160:167], %[idx]\n\t"
          "v_exp_f32 v120, v120\n\t"
          "v_exp_f32 v121, v121\n\t"
          "v_exp_f32 v122, v122\n\t"
          "v_exp_f32 v123, v123\n\t"
          "v_smfmac_f32_32x32x32_bf16 %[ob], %[v11], v[168:175], %[idx]\n\t"
          "s_waitcnt lgkmcnt(0)\n\t"
          "v_exp_f32 v124, v124\n\t"
          "v_exp_f32 v125, v125\n\t"
          "v_exp_f32 v126, v126\n\t"
          "v_exp_f32 v127, v127\n\t"
          "v_mfma_f32_32x32x8_bf16 v[144:159], v[176:177], %[qa8e], 0\n\t"
          "v_exp_f32 v128, v128\n\t"
          "v_exp_f32 v129, v129\n\t"
          "v_exp_f32 v130, v130\n\t"
          "v_exp_f32 v131, v131\n\t"
          "v_mfma_f32_32x32x16_bf16 v[144:159], %[k1], %[qa16], v[144:159]\n\t"
          "v_exp_f32 v132, v132\n\t"
          "v_exp_f32 v133, v133\n\t"
          "v_exp_f32 v134, v134\n\t"
          "v_exp_f32 v135, v135\n\t"
          "v_smfmac_f32_32x32x32_bf16 %[oa], %[v00], v[112:119], %[idx]\n\t"
          "ds_read_b128 %[k0], %[ak] offset:4416\n\t"
          "v_exp_f32 v136, v136\n\t"
          "v_exp_f32 v137, v137\n\t"
          "v_exp_f32 v138, v138\n\t"
          "v_exp_f32 v139, v139\n\t"
          "v_smfmac_f32_32x32x32_bf16 %[oa], %[v10], v[120:127], %[idx]\n\t"
          "ds_read_b128 %[v01], %[av] offset:2368\n\t"
          "ds_read_b128 %[v11], %[av] offset:3040\n\t"
          "v_exp_f32 v140, v140\n\t"
          "v_exp_f32 v141, v141\n\t"
          "v_exp_f32 v142, v142\n\t"
          "v_exp_f32 v143, v143\n\t"
          "v_mfma_f32_32x32x8_bf16 v[160:175], v[176:177], %[qb8e], 0\n\t"
          "v_exp_f32 v144, v144\n\t"
          "v_exp_f32 v145, v145\n\t"
          "v_exp_f32 v146, v146\n\t"
          "v_exp_f32 v147, v147\n\t"
          "v_mfma_f32_32x32x16_bf16 v[160:175], %[k1], %[qb16], v[160:175]\n\t"
          "v_exp_f32 v148, v148\n\t"
          "v_exp_f32 v149, v149\n\t"
          "v_exp_f32 v150, v150\n\t"
          "v_exp_f32 v151, v151\n\t"
          "v_smfmac_f32_32x32x32_bf16 %[ob], %[v00], v[128:135], %[idx]\n\t"
          "v_exp_f32 v152, v152\n\t"
          "v_exp_f32 v153, v153\n\t"
          "v_exp_f32 v154, v154\n\t"
          "v_exp_f32 v155, v155\n\t"
          "v_smfmac_f32_32x32x32_bf16 %[ob], %[v10], v[136:143], %[idx]\n\t"
          "s_waitcnt lgkmcnt(0)\n\t"
          "v_exp_f32 v156, v156\n\t"
          "v_exp_f32 v157, v157\n\t"
          "v_exp_f32 v158, v158\n\t"
          "v_exp_f32 v159, v159\n\t"
          "v_mfma_f32_32x32x8_bf16 v[112:127], v[176:177], %[qa8o], 0\n\t"
          "v_exp_f32 v160, v160\n\t"
          "v_exp_f32 v161, v161\n\t"
          "v_exp_f32 v162, v162\n\t"
          "v_exp_f32 v163, v163\n\t"
          "v_mfma_f32_32x32x16_bf16 v[112:127], %[k0], %[qa16], v[112:127]\n\t"
          "v_exp_f32 v164, v164\n\t"
          "v_exp_f32 v165, v165\n\t"
          "v_exp_f32 v166, v166\n\t"
          "v_exp_f32 v167, v167\n\t"
          "v_smfmac_f32_32x32x32_bf16 %[oa], %[v01], v[144:151], %[idx]\n\t"
          "ds_read_b128 %[k1], %[ak] offset:6784\n\t"
          "v_exp_f32 v168, v168\n\t"
          "v_exp_f32 v169, v169\n\t"
          "v_exp_f32 v170, v170\n\t"
          "v_exp_f32 v171, v171\n\t"
          "v_smfmac_f32_32x32x32_bf16 %[oa], %[v11], v[152:159], %[idx]\n\t"
          "ds_read_b128 %[v00], %[av] offset:4736\n\t"
          "ds_read_b128 %[v10], %[av] offset:5408\n\t"
          "v_exp_f32 v172, v172\n\t"
          "v_exp_f32 v173, v173\n\t"
          "v_exp_f32 v174, v174\n\t"
          "v_exp_f32 v175, v175\n\t"
          "v_mfma_f32_32x32x8_bf16 v[128:143], v[176:177], %[qb8o], 0\n\t"
          "v_exp_f32 v112, v112\n\t"
          "v_exp_f32 v113, v113\n\t"
          "v_exp_f32 v114, v114\n\t"
          "v_exp_f32 v115, v115\n\t"
          "v_mfma_f32_32x32x16_bf16 v[128:143], %[k0], %[qb16], v[128:143]\n\t"
          "v_exp_f32 v116, v116\n\t"
          "v_exp_f32 v117, v117\n\t"
          "v_exp_f32 v118, v118\n\t"
          "v_exp_f32 v119, v119\n\t"
          "v_smfmac_f32_32x32x32_bf16 %[ob], %[v01], v[160:167], %[idx]\n\t"
          "v_exp_f32 v120, v120\n\t"
          "v_exp_f32 v121, v121\n\t"
          "v_exp_f32 v122, v122\n\t"
          "v_exp_f32 v123, v123\n\t"
          "v_smfmac_f32_32x32x32_bf16 %[ob], %[v11], v[168:175], %[idx]\n\t"
          "s_waitcnt lgkmcnt(0)\n\t"
          "v_exp_f32 v124, v124\n\t"
          "v_exp_f32 v125, v125\n\t"
          "v_exp_f32 v126, v126\n\t"
          "v_exp_f32 v127, v127\n\t"
          "v_mfma_f32_32x32x8_bf16 v[144:159], v[178:179], %[qa8e], 0\n\t"
          "v_exp_f32 v128, v128\n\t"
          "v_exp_f32 v129, v129\n\t"
          "v_exp_f32 v130, v130\n\t"
          "v_exp_f32 v131, v131\n\t"
          "v_mfma_f32_32x32x16_bf16 v[144:159], %[k1], %[qa16], v[144:159]\n\t"
          "v_exp_f32 v132, v132\n\t"
          "v_exp_f32 v133, v133\n\t"
          "v_exp_f32 v134, v134\n\t"
          "v_exp_f32 v135, v135\n\t"
          "v_smfmac_f32_32x32x32_bf16 %[oa], %[v00], v[112:119], %[idx]\n\t"
          "ds_read_b128 %[k0], %[ak] offset:9152\n\t"
          "v_exp_f32 v136, v136\n\t"
          "v_exp_f32 v137, v137\n\t"
          "v_exp_f32 v138, v138\n\t"
          "v_exp_f32 v139, v139\n\t"
          "v_smfmac_f32_32x32x32_bf16 %[oa], %[v10], v[120:127], %[idx]\n\t"
          "ds_read_b128 %[v01], %[av] offset:7104\n\t"
          "ds_read_b128 %[v11], %[av] offset:7776\n\t"
          "v_exp_f32 v140, v140\n\t"
          "v_exp_f32 v141, v141\n\t"
          "v_exp_f32 v142, v142\n\t"
          "v_exp_f32 v143, v143\n\t"
          "v_mfma_f32_32x32x8_bf16 v[160:175], v[178:179], %[qb8e], 0\n\t"
          "v_exp_f32 v144, v144\n\t"
          "v_exp_f32 v145, v145\n\t"
          "v_exp_f32 v146, v146\n\t"
          "v_exp_f32 v147, v147\n\t"
          "v_mfma_f32_32x32x16_bf16 v[160:175], %[k1], %[qb16], v[160:175]\n\t"
          "v_exp_f32 v148, v148\n\t"
          "v_exp_f32 v149, v149\n\t"
          "v_exp_f32 v150, v150\n\t"
          "v_exp_f32 v151, v151\n\t"
          "v_smfmac_f32_32x32x32_bf16 %[ob], %[v00], v[128:135], %[idx]\n\t"
          "v_exp_f32 v152, v152\n\t"
          "v_exp_f32 v153, v153\n\t"
          "v_exp_f32 v154, v154\n\t"
          "v_exp_f32 v155, v155\n\t"
          "v_smfmac_f32_32x32x32_bf16 %[ob], %[v10], v[136:143], %[idx]\n\t"
          "s_waitcnt lgkmcnt(0)\n\t"
          "v_exp_f32 v156, v156\n\t"
          "v_exp_f32 v157, v157\n\t"
          "v_exp_f32 v158, v158\n\t"
          "v_exp_f32 v159, v159\n\t"
          "v_mfma_f32_32x32x8_bf16 v[112:127], v[178:179], %[qa8o], 0\n\t"
          "v_exp_f32 v160, v160\n\t"
          "v_exp_f32 v161, v161\n\t"
          "v_exp_f32 v162, v162\n\t"
          "v_exp_f32 v163, v163\n\t"
          "v_mfma_f32_32x32x16_bf16 v[112:127], %[k0], %[qa16], v[112:127]\n\t"
          "v_exp_f32 v164, v164\n\t"
          "v_exp_f32 v165, v165\n\t"
          "v_exp_f32 v166, v166\n\t"
          "v_exp_f32 v167, v167\n\t"
          "v_smfmac_f32_32x32x32_bf16 %[oa], %[v01], v[144:151], %[idx]\n\t"
          "ds_read_b128 %[k1], %[ak] offset:11520\n\t"
          "ds_read_b128 v[180:183], %[ak] offset:22016\n\t"
          "v_exp_f32 v168, v168\n\t"
          "v_exp_f32 v169, v169\n\t"
          "v_exp_f32 v170, v170\n\t"
          "v_exp_f32 v171, v171\n\t"
          "v_smfmac_f32_32x32x32_bf16 %[oa], %[v11], v[152:159], %[idx]\n\t"
          "ds_read_b128 %[v00], %[av] offset:9472\n\t"
          "ds_read_b128 %[v10], %[av] offset:10144\n\t"
          "v_exp_f32 v172, v172\n\t"
          "v_exp_f32 v173, v173\n\t"
          "v_exp_f32 v174, v174\n\t"
          "v_exp_f32 v175, v175\n\t"
          "v_mfma_f32_32x32x8_bf16 v[128:143], v[178:179], %[qb8o], 0\n\t"
          "v_exp_f32 v112, v112\n\t"
          "v_exp_f32 v113, v113\n\t"
          "v_exp_f32 v114, v114\n\t"
          "v_exp_f32 v115, v115\n\t"
          "v_mfma_f32_32x32x16_bf16 v[128:143], %[k0], %[qb16], v[128:143]\n\t"
          "v_exp_f32 v116, v116\n\t"
          "v_exp_f32 v117, v117\n\t"
          "v_exp_f32 v118, v118\n\t"
          "v_exp_f32 v119, v119\n\t"
          "v_smfmac_f32_32x32x32_bf16 %[ob], %[v01], v[160:167], %[idx]\n\t"
          "v_exp_f32 v120, v120\n\t"
          "v_exp_f32 v121, v121\n\t"
          "v_exp_f32 v122, v122\n\t"
          "v_exp_f32 v123, v123\n\t"
          "v_smfmac_f32_32x32x32_bf16 %[ob], %[v11], v[168:175], %[idx]\n\t"
          "s_waitcnt lgkmcnt(0)\n\t"
          "v_exp_f32 v124, v124\n\t"
          "v_exp_f32 v125, v125\n\t"
          "v_exp_f32 v126, v126\n\t"
          "v_exp_f32 v127, v127\n\t"
          "v_mfma_f32_32x32x8_bf16 v[144:159], v[180:181], %[qa8e], 0\n\t"
          "v_exp_f32 v128, v128\n\t"
          "v_exp_f32 v129, v129\n\t"
          "v_exp_f32 v130, v130\n\t"
          "v_exp_f32 v131, v131\n\t"
          "v_mfma_f32_32x32x16_bf16 v[144:159], %[k1], %[qa16], v[144:159]\n\t"
          "v_exp_f32 v132, v132\n\t"
          "v_exp_f32 v133, v133\n\t"
          "v_exp_f32 v134, v134\n\t"
          "v_exp_f32 v135, v135\n\t"
          "v_smfmac_f32_32x32x32_bf16 %[oa], %[v00], v[112:119], %[idx]\n\t"
          "ds_read_b128 %[k0], %[ak] offset:13888\n\t"
          "v_exp_f32 v136, v136\n\t"
          "v_exp_f32 v137, v137\n\t"
          "v_exp_f32 v138, v138\n\t"
          "v_exp_f32 v139, v139\n\t"
          "v_smfmac_f32_32x32x32_bf16 %[oa], %[v10], v[120:127], %[idx]\n\t"
          "ds_read_b128 %[v01], %[av] offset:11840\n\t"
          "ds_read_b128 %[v11], %[av] offset:12512\n\t"
          "v_exp_f32 v140, v140\n\t"
          "v_exp_f32 v141, v141\n\t"
          "v_exp_f32 v142, v142\n\t"
          "v_exp_f32 v143, v143\n\t"
          "v_mfma_f32_32x32x8_bf16 v[160:175], v[180:181], %[qb8e], 0\n\t"
          "v_exp_f32 v144, v144\n\t"
          "v_exp_f32 v145, v145\n\t"
          "v_exp_f32 v146, v146\n\t"
          "v_exp_f32 v147, v147\n\t"
          "v_mfma_f32_32x32x16_bf16 v[160:175], %[k1], %[qb16], v[160:175]\n\t"
          "v_exp_f32 v148, v148\n\t"
          "v_exp_f32 v149, v149\n\t"
          "v_exp_f32 v150, v150\n\t"
          "v_exp_f32 v151, v151\n\t"
          "v_smfmac_f32_32x32x32_bf16 %[ob], %[v00], v[128:135], %[idx]\n\t"
          "v_exp_f32 v152, v152\n\t"
          "v_exp_f32 v153, v153\n\t"
          "v_exp_f32 v154, v154\n\t"
          "v_exp_f32 v155, v155\n\t"
          "v_smfmac_f32_32x32x32_bf16 %[ob], %[v10], v[136:143], %[idx]\n\t"
          "s_waitcnt lgkmcnt(0)\n\t"
          "v_exp_f32 v156, v156\n\t"
          "v_exp_f32 v157, v157\n\t"
          "v_exp_f32 v158, v158\n\t"
          "v_exp_f32 v159, v159\n\t"
          "v_mfma_f32_32x32x8_bf16 v[112:127], v[180:181], %[qa8o], 0\n\t"
          "v_exp_f32 v160, v160\n\t"
          "v_exp_f32 v161, v161\n\t"
          "v_exp_f32 v162, v162\n\t"
          "v_exp_f32 v163, v163\n\t"
          "v_mfma_f32_32x32x16_bf16 v[112:127], %[k0], %[qa16], v[112:127]\n\t"
          "v_exp_f32 v164, v164\n\t"
          "v_exp_f32 v165, v165\n\t"
          "v_exp_f32 v166, v166\n\t"
          "v_exp_f32 v167, v167\n\t"
          "v_smfmac_f32_32x32x32_bf16 %[oa], %[v01], v[144:151], %[idx]\n\t"
          "ds_read_b128 %[k1], %[ak] offset:16256\n\t"
          "v_exp_f32 v168, v168\n\t"
          "v_exp_f32 v169, v169\n\t"
          "v_exp_f32 v170, v170\n\t"
          "v_exp_f32 v171, v171\n\t"
          "v_smfmac_f32_32x32x32_bf16 %[oa], %[v11], v[152:159], %[idx]\n\t"
          "ds_read_b128 %[v00], %[av] offset:14208\n\t"
          "ds_read_b128 %[v10], %[av] offset:14880\n\t"
          "v_exp_f32 v172, v172\n\t"
          "v_exp_f32 v173, v173\n\t"
          "v_exp_f32 v174, v174\n\t"
          "v_exp_f32 v175, v175\n\t"
          "v_mfma_f32_32x32x8_bf16 v[128:143], v[180:181], %[qb8o], 0\n\t"
          "v_exp_f32 v112, v112\n\t"
          "v_exp_f32 v113, v113\n\t"
          "v_exp_f32 v114, v114\n\t"
          "v_exp_f32 v115, v115\n\t"
          "v_mfma_f32_32x32x16_bf16 v[128:143], %[k0], %[qb16], v[128:143]\n\t"
          "s_sub_u32 %[cb], %[cb], 1\n\t"
          "v_exp_f32 v116, v116\n\t"
          "v_exp_f32 v117, v117\n\t"
          "v_exp_f32 v118, v118\n\t"
          "v_exp_f32 v119, v119\n\t"
          "v_smfmac_f32_32x32x32_bf16 %[ob], %[v01], v[160:167], %[idx]\n\t"
          "v_exp_f32 v120, v120\n\t"
          "v_exp_f32 v121, v121\n\t"
          "v_exp_f32 v122, v122\n\t"
          "v_exp_f32 v123, v123\n\t"
          "v_smfmac_f32_32x32x32_bf16 %[ob], %[v11], v[168:175], %[idx]\n\t"
          "s_waitcnt lgkmcnt(0)\n\t"
          "v_exp_f32 v124, v124\n\t"
          "v_exp_f32 v125, v125\n\t"
          "v_exp_f32 v126, v126\n\t"
          "v_exp_f32 v127, v127\n\t"
          "v_mfma_f32_32x32x8_bf16 v[144:159], v[182:183], %[qa8e], 0\n\t"
          "v_exp_f32 v128, v128\n\t"
          "v_exp_f32 v129, v129\n\t"
          "v_exp_f32 v130, v130\n\t"
          "v_exp_f32 v131, v131\n\t"
          "v_mfma_f32_32x32x16_bf16 v[144:159], %[k1], %[qa16], v[144:159]\n\t"
          "s_cmp_lg_u32 %[cb], 0\n\t"
          "v_exp_f32 v132, v132\n\t"
          "v_exp_f32 v133, v133\n\t"
          "v_exp_f32 v134, v134\n\t"
          "v_exp_f32 v135, v135\n\t"
          "v_smfmac_f32_32x32x32_bf16 %[oa], %[v00], v[112:119], %[idx]\n\t"
          "ds_read_b128 %[k0], %[ak] offset:18624\n\t"
          "v_exp_f32 v136, v136\n\t"
          "v_exp_f32 v137, v137\n\t"
          "v_exp_f32 v138, v138\n\t"
          "v_exp_f32 v139, v139\n\t"
          "v_smfmac_f32_32x32x32_bf16 %[oa], %[v10], v[120:127], %[idx]\n\t"
          "ds_read_b128 %[v01], %[av] offset:16576\n\t"
          "ds_read_b128 %[v11], %[av] offset:17248\n\t"
          "v_exp_f32 v140, v140\n\t"
          "v_exp_f32 v141, v141\n\t"
          "v_exp_f32 v142, v142\n\t"
          "v_exp_f32 v143, v143\n\t"
          "v_mfma_f32_32x32x8_bf16 v[160:175], v[182:183], %[qb8e], 0\n\t"
          "v_exp_f32 v144, v144\n\t"
          "v_exp_f32 v145, v145\n\t"
          "v_exp_f32 v146, v146\n\t"
          "v_exp_f32 v147, v147\n\t"
          "v_mfma_f32_32x32x16_bf16 v[160:175], %[k1], %[qb16], v[160:175]\n\t"
          "v_add_u32 %[ak], %[dl], %[ak]\n\t"
          "v_add_u32 %[av], %[dl], %[av]\n\t"
          "v_exp_f32 v148, v148\n\t"
          "v_exp_f32 v149, v149\n\t"
          "v_exp_f32 v150, v150\n\t"
          "v_exp_f32 v151, v151\n\t"
          "v_smfmac_f32_32x32x32_bf16 %[ob], %[v00], v[128:135], %[idx]\n\t"
          "s_mul_i32 %[dl], %[dl], -1\n\t"
          "v_exp_f32 v152, v152\n\t"
          "v_exp_f32 v153, v153\n\t"
          "v_exp_f32 v154, v154\n\t"
          "v_exp_f32 v155, v155\n\t"
          "v_smfmac_f32_32x32x32_bf16 %[ob], %[v10], v[136:143], %[idx]\n\t"
          "s_waitcnt lgkmcnt(0)\n\t"
          "s_cbranch_scc0 7f\n\t"
          "s_barrier\n\t"
          "7:\n\t"
          "v_exp_f32 v156, v156\n\t"
          "v_exp_f32 v157, v157\n\t"
          "v_exp_f32 v158, v158\n\t"
          "v_exp_f32 v159, v159\n\t"
          "s_cbranch_scc1 3b\n\t"
          "v_exp_f32 v160, v160\n\t"
          "v_exp_f32 v161, v161\n\t"
          "v_exp_f32 v162, v162\n\t"
          "v_exp_f32 v163, v163\n\t"
          "v_exp_f32 v164, v164\n\t"
          "v_exp_f32 v165, v165\n\t"
          "v_exp_f32 v166, v166\n\t"
          "v_exp_f32 v167, v167\n\t"
          "v_smfmac_f32_32x32x32_bf16 %[oa], %[v01], v[144:151], %[idx]\n\t"
          "v_exp_f32 v168, v168\n\t"
          "v_exp_f32 v169, v169\n\t"
          "v_exp_f32 v170, v170\n\t"
          "v_exp_f32 v171, v171\n\t"
          "v_smfmac_f32_32x32x32_bf16 %[oa], %[v11], v[152:159], %[idx]\n\t"
          "v_exp_f32 v172, v172\n\t"
          "v_exp_f32 v173, v173\n\t"
          "v_exp_f32 v174, v174\n\t"
          "v_exp_f32 v175, v175\n\t"
          "s_nop 1\n\t"
          "v_smfmac_f32_32x32x32_bf16 %[ob], %[v01], v[160:167], %[idx]\n\t"
          "v_smfmac_f32_32x32x32_bf16 %[ob], %[v11], v[168:175], %[idx]\n\t"
          "s_nop 7\n\t"
          "s_nop 7\n\t"
          : [oa] "+v"(oa), [ob] "+v"(ob), [k0] "=&v"(k0), [k1] "=&v"(k1), [v00] "+v"(v00), [v10] "+v"(v10), [v01] "+v"(v01),
            [v11] "+v"(v11), [ak] "+v"(ak), [av] "+v"(av), [fa] "+v"(fa), [fb] "+v"(fb), [ub] "+v"(ub), [ca] "+s"(ca),
            [cb] "+s"(cb), [dl] "+s"(dl)
          : [qa16] "v"(qa16), [qb16] "v"(qb16), [qa8e] "v"(qa8e), [qa8o] "v"(qa8o), [qb8e] "v"(qb8e), [qb8o] "v"(qb8o),
            [idx] "v"(idx), [yd] "s"(L_KC + wave * KC_W), [yf] "s"(M_YRDY + wave * 4)
          : "scc", "memory",
            "v112", "v113", "v114", "v115", "v116", "v117", "v118", "v119", "v120", "v121", "v122", "v123",
            "v124", "v125", "v126", "v127", "v128", "v129", "v130", "v131", "v132", "v133", "v134", "v135",
            "v136", "v137", "v138", "v139", "v140", "v141", "v142", "v143", "v144", "v145", "v146", "v147",
            "v148", "v149", "v150", "v151", "v152", "v153", "v154", "v155", "v156", "v157", "v158", "v159",
            "v160", "v161", "v162", "v163", "v164", "v165", "v166", "v167", "v168", "v169", "v170", "v171",
            "v172", "v173", "v174", "v175", "v176", "v177", "v178", "v179", "v180", "v181", "v182", "v183",
            "v184", "v185", "v186", "v187", "v188", "v189", "v190", "v191", "v192", "v193", "v194", "v195",
            "v196", "v197", "v198", "v199", "v200", "v201", "v202", "v203", "v204", "v205", "v206", "v207",
            "v208", "v209", "v210", "v211", "v212", "v213", "v214", "v215", "v216", "v217", "v218", "v219",
            "v220", "v221", "v222", "v223", "v224", "v225", "v226", "v227", "v228", "v229", "v230", "v231",
            "v232", "v233", "v234", "v235", "v236", "v237", "v238", "v239", "v240", "v241", "v242", "v243",
            "v244", "v245", "v246", "v247"
      );
    } else {
      __builtin_amdgcn_s_setprio(0);
      wait_vm<0>();
      hw_barrier();
      float ma = -__builtin_inff(), mb = ma;
      f32x16 y0 = zero16(), y1 = zero16();
      const unsigned yd = L_KC + wave * KC_W + r * 64 + h * 8;
      u32x4 kc = lds128(sm, ak);
      unsigned k8c = *(LAS unsigned*)(sm + ak + 1024 + 12);
      ak += 2048;
#pragma unroll 1
      for (int ch = 0; ch < 8; ++ch) {
        const unsigned so = (ch & 1) * CHUNK;
#pragma unroll 1
        for (int t = 0; t < 8; ++t) {
          u32x4 kn = lds128(sm, ak + so + t * REC);
          unsigned k8n = *(LAS unsigned*)(sm + ak + so + QUAD_OFF + (t >> 2) * 1024 + (t & 3) * 4);
          v00 = lds128(sm, av + so + t * REC);
          v10 = lds128(sm, av + so + t * REC + V1_OFF);
          attn_tile_guarded(kc, k8c, qa16, qa8, v00, v10, ma, oa);
          attn_tile_guarded(kc, k8c, qb16, qb8, v00, v10, mb, ob);
          kc = kn; k8c = k8n;
          if (ssm && ch < 4) { conv_step(sm, fa, ub, y0, y1); fa += 64; ub -= UCOL; }
        }
        if (ssm && ch == 6) {
#pragma unroll
          for (int i = 0; i < 4; ++i) {
            *(LAS u32x2*)(sm + yd + i * 16) = u32x2{pk_bf(y0[4 * i], y0[4 * i + 1]), pk_bf(y0[4 * i + 2], y0[4 * i + 3])};
            *(LAS u32x2*)(sm + yd + 2048 + i * 16) = u32x2{pk_bf(y1[4 * i], y1[4 * i + 1]), pk_bf(y1[4 * i + 2], y1[4 * i + 3])};
          }
          wait_lgkm0();
          lds_st(sm, M_YRDY + wave * 4, 1);
        }
        if (ch < 7) hw_barrier();
      }
    }
    STAMP(17);
    const unsigned l2 = opaque(threadIdx.x), ov = o_lane(l2);
    const bool hi = l2 & 32;
    const float ia = __builtin_amdgcn_rcpf(oa[12]), ib = __builtin_amdgcn_rcpf(ob[12]);
    const unsigned om = ov + o_main(c, j, wave), ol = ov + o_left(c, j) + wave * 40960;
    const unsigned lo_ = L_FIRST + (l2 & 63) * 16;
    *(LAS unsigned*)(sm + lo_ + wave * 4) = pk_bf((hi ? oa[10] : oa[8]) * ia, (hi ? oa[11] : oa[9]) * ia);
    *(LAS unsigned*)(sm + lo_ + 1024 + wave * 4) = pk_bf((hi ? ob[10] : ob[8]) * ib, (hi ? ob[11] : ob[9]) * ib);
    lds_arrive_nowait<M_CNT_L>();
    __builtin_amdgcn_raw_buffer_store_b128(u32x4{pk_bf(oa[0] * ia, oa[1] * ia), pk_bf(oa[2] * ia, oa[3] * ia), pk_bf(oa[4] * ia, oa[5] * ia), pk_bf(oa[6] * ia, oa[7] * ia)}, wsr, om, 0, 16);
    __builtin_amdgcn_raw_buffer_store_b128(u32x4{pk_bf(ob[0] * ib, ob[1] * ib), pk_bf(ob[2] * ib, ob[3] * ib), pk_bf(ob[4] * ib, ob[5] * ib), pk_bf(ob[6] * ib, ob[7] * ib)}, wsr, om + 40960, 0, 16);
    if (wave < 2) {
      lds_wait_ge<false>(sm, M_CNT_L, 4);
      __builtin_amdgcn_raw_buffer_store_b128(lds128(sm, lo_ + wave * 1024), wsr, ol, 0, 16);
    }
    wait_vm<0>();
    if (l2 == (unsigned)wave * 64) gst64(P.ws + WS_FO + (c * 32 + j * 4 + wave) * FL, tag);
    STAMP(18);
  } else {
#define SPRIO0 0
    if (ssm) __builtin_amdgcn_s_setprio(SPRIO0); else __builtin_amdgcn_s_setprio(3);
    const int sw = wave - 4;
    const unsigned base = L_KC + sw * KC_W;
    const int sblk = cl * 5 + j, d = bx * 80 + sblk * 4 + sw;
    if (ssm) {
      ssm_kernel_gen(P, sm, base, d, lane, ep, tag STAMP_PASS);
      STAMP(2);
    } else if (j < 7) {
      kv_project(P, sm, 2 * c + (j - 5), sw, lane, ep, tag, wsr STAMP_PASS);
    } else {
      hw_barrier();
      wait_vm<0>();
      tag = make_tag(ep);
    }
    STAMP(4);
    __builtin_amdgcn_s_setprio(3);
    hw_barrier();

    const char* wb = (const char*)P.wo + j * 160 + (size_t)sw * 160 * ROWB;
    unsigned vo[5];
#pragma unroll
    for (int q = 0; q < 5; ++q) { const unsigned i = q * 64 + lane; vo[q] = (i / 10) * ROWB + (i % 10) * 16; }
    auto wo_rows = [&](int a) __attribute__((always_inline)) {
#pragma unroll
      for (int q = 0; q < 5; ++q) dma16<0, false>(wb + a * 32 * ROWB, vo[q], L_WO + sw * 25600 + (a * 5 + q) * 1024);
    };
#pragma unroll
    for (int seg = 0; seg < 8; ++seg) {
      if (seg < 7) dma_chunk(P, seg + 1, sw, lane);
      if (seg == 0 && sw == 0 && lane == 0) gst64(P.ws + WS_ARR + blockIdx.x * 8, tag);
      if (!ssm) {
        if (seg >= 1 && seg < 6) wo_rows(seg - 1);
        if (seg == 6 && blockIdx.x == 255 && sw == 3) {
#pragma unroll 1
          for (int q = 0; q < 4; ++q) poll_tags(P.ws + WS_ARR + (q * 64 + lane) * 8, true, tag);
          if (lane == 0) gst64(P.ws + WS_EPOCH, tag);
        }
        wait_vm<0>();
      } else if (seg == 6) {
        __builtin_amdgcn_s_sleep(12);
        lds_wait_ge<true>(sm, M_YRDY + sw * 4, 1);
        u32x4 av4[4];
#pragma unroll
        for (int q = 0; q < 4; ++q) av4[q] = lds128(sm, base + q * 1024 + lane * 16);
#pragma unroll
        for (int q = 0; q < 4; ++q) __builtin_amdgcn_raw_buffer_store_b128(av4[q], wsr, WS_AT + d * 4096 + q * 1024 + lane * 16, 0, 16);
        lds_arrive<M_CNT_S>();
        lds_wait_ge<true>(sm, M_CNT_S, 4);
        wo_rows(0);
        wait_vm<9>();
      } else if (seg == 7) {
        wo_rows(1);
        wait_vm<10>();
        if (lds_ticket<M_CNT_S2>() == 3 && lane < 8) gst64(P.ws + WS_FA + (lane * 256 + bx * 32 + sblk) * FL, tag);
        wo_rows(2); wo_rows(3); wo_rows(4);
        const u64 fl = gld64(P.ws + WS_FA + (bx * 256 + j * 32 + min(lane, 19)) * FL);
        wait_vm<0>();
        if (__builtin_amdgcn_ballot_w64(fl != tag) == 0) {
          asm volatile("" ::: "memory");
          a_todo = false;
          fetch_a(P, c, j, sw, lane);
        }
      } else {
        wait_vm<0>();
        if (seg == 1) {
#pragma unroll
          for (int q = 0; q < 5; ++q) touch(wb, ((q * 64 + lane) >> 1) * ROWB + (lane & 1) * 144);
        }
      }
      STAMP(8 + 2 * seg);
      if (seg < 7) {
        hw_barrier();
        STAMP(9 + 2 * seg);
      }
    }
    lds_arrive_nowait<M_WO>();
    __builtin_amdgcn_s_setprio(0);
  }

  {
    const unsigned lane = opaque(threadIdx.x) & 63, g = lane >> 4, q4 = (lane & 15) >> 2, p4 = lane & 3;
    const int tt = wave & 3, kh = wave >> 2;
    const unsigned wm = opaque(L_WO + kh * 51200 + (20 * (g >> 1) + 4 * (g & 1) + q4) * 160 + p4 * 8);
    const unsigned wl = opaque(L_WO + kh * 51200 + (80 * (g >> 1) + 20 * (q4 >> 1) + 16 + 2 * (g & 1) + (q4 & 1)) * 160 + p4 * 8);
    const int rot = 2 * kh;
    const unsigned ofs = WS_OF + ((4 * c + tt) * 20 + kh * 10) * 1024;
    auto fetch = [&](int i) __attribute__((always_inline)) {
      return __builtin_amdgcn_raw_buffer_load_b128(wsr, lane * 16, ofs + (i < 8 ? (i + rot) & 7 : i) * 1024, 16);
    };
    lds_wait_ge<false>(sm, M_WO, 4);
    STAMP(1);
    u32x4 wf[8][5];
#pragma unroll
    for (int i = 0; i < 8; i += 2) {
      const unsigned wi = wm + ((i + rot) & 7) * 6400;
#pragma unroll
      for (int e = 0; e < 2; ++e)
#pragma unroll
        for (int nt = 0; nt < 5; ++nt) wf[i + e][nt] = cat(lds_tr(sm, wi + e * 6400 + nt * 32), lds_tr(sm, wi + e * 6400 + nt * 32 + 1280));
    }
    const float zs = *(LAS float*)(sm + L_Z + (tt * 16 + (lane & 15)) * 4);
    float gt = rbf(__builtin_amdgcn_rcpf(1.f + __builtin_amdgcn_exp2f(-rbf(zs) * LOG2E)));
    float om = rbf(1.f - gt);
    unsigned ex = L_RING + tt * 5120 + lane * 16, aa = L_ATILE + (4 * g + q4) * 128 + tt * 32 + p4 * 8;
    unsigned st = L_RING + CHUNK + (tt * 16 + (lane & 15)) * 176 + g * 8;
    const unsigned it = wave * 80 + lane, row = (it * 6554) >> 16, row2 = ((it + 64) * 6554) >> 16;
    unsigned sr = L_RING + CHUNK + it * 16 + row * 16, sr2 = L_RING + CHUNK + (it + 64) * 16 + row2 * 16;
    unsigned oo = (c * 64 + row) * ROWB + j * 160 + (it - row * 10) * 16, oo2 = (c * 64 + row2) * ROWB + j * 160 + (it + 64 - row2 * 10) * 16;
    asm volatile("" : "+v"(gt), "+v"(om), "+v"(ex), "+v"(aa), "+v"(st), "+v"(sr), "+v"(sr2), "+v"(oo), "+v"(oo2));
    STAMP(24);
    u32x4 of[10];
    f32x4 acc[5];
#pragma unroll
    for (int nt = 0; nt < 5; ++nt) acc[nt] = f32x4{0.f, 0.f, 0.f, 0.f};
    const char* fo = P.ws + WS_FO + (c * 32 + 16 * kh + ((lane + 2 * rot) & 15)) * FL;
    unsigned ready = 0;
    auto wait_for = [&](unsigned need) __attribute__((always_inline)) {
#pragma unroll 1
      for (unsigned n = 0; n < SPIN_CAP && (ready & need) != need; ++n) {
        if (n) __builtin_amdgcn_s_sleep(1);
        bool ok = false;
        if (lane < 16 && !(ready >> lane & 1)) ok = gld64(fo) == tag;
        ready |= (unsigned)__builtin_amdgcn_ballot_w64(ok);
      }
      asm volatile("" ::: "memory");
    };
    auto mul = [&](int i) __attribute__((always_inline)) {
#pragma unroll
      for (int nt = 0; nt < 5; ++nt) acc[nt] = mfma16(wf[i][nt], of[i], acc[nt]);
    };
    if (kh == 0) {
      wait_for(0xffff);
      STAMP(25);
#pragma unroll
      for (int i = 0; i < 10; ++i) of[i] = fetch(i);
      sched_fence();
#pragma unroll
      for (int i = 0; i < 6; ++i) mul(i);
    } else {
      wait_for(0xfff);
#pragma unroll
      for (int i = 0; i < 6; ++i) of[i] = fetch(i);
      sched_fence();
#pragma unroll
      for (int i = 0; i < 6; ++i) mul(i);
      if (a_todo) {
        poll_tags(P.ws + WS_FA + (bx * 256 + j * 32 + lane) * FL, lane < 20, tag);
        fetch_a(P, c, j, tt, lane);
      }
      wait_for(0xffff);
      STAMP(25);
#pragma unroll
      for (int i = 6; i < 10; ++i) of[i] = fetch(i);
      sched_fence();
    }
    u32x4 wl0[5], wl1[5];
#pragma unroll
    for (int nt = 0; nt < 5; ++nt) {
      wl0[nt] = cat(lds_tr(sm, wl + nt * 32), lds_tr(sm, wl + nt * 32 + 6400));
      wl1[nt] = cat(lds_tr(sm, wl + 25600 + nt * 32), lds_tr(sm, wl + 25600 + nt * 32 + 6400));
    }
    sched_fence();
    mul(6); mul(7);
#pragma unroll
    for (int nt = 0; nt < 5; ++nt) acc[nt] = mfma16(wl0[nt], of[8], acc[nt]);
#pragma unroll
    for (int nt = 0; nt < 5; ++nt) acc[nt] = mfma16(wl1[nt], of[9], acc[nt]);
    STAMP(26);
#pragma unroll
    for (int nt = 0; nt < 5; ++nt)
      if ((nt >= 3) != (kh == 1)) *(LAS f32x4*)(sm + ex + nt * 1024) = acc[nt];
    wait_vm<0>();
    hw_barrier();
    STAMP(27);
    const __amdgpu_buffer_rsrc_t outr = __builtin_amdgcn_make_buffer_rsrc((void*)P.out, 0, SEQ * ROWB, 0x00020000);
    auto finish = [&](auto first, auto count) __attribute__((always_inline)) {
      constexpr int F = decltype(first)::value, N = decltype(count)::value;
      f32x4 b[N];
      u32x2 a[N];
#pragma unroll
      for (int k = 0; k < N; ++k) { b[k] = *(LAS f32x4*)(sm + ex + (F + k) * 1024); a[k] = lds_tr(sm, aa + (F + k) * 2048); }
      sched_fence();
#pragma unroll
      for (int k = 0; k < N; ++k) {
        const f32x4 t = (acc[F + k] + b[k]) * om;
        const float a0 = bf2f(a[k].x & 0xffffu), a1 = bf_hi(a[k].x), a2 = bf2f(a[k].y & 0xffffu), a3 = bf_hi(a[k].y);
        *(LAS u32x2*)(sm + st + (F + k) * 32) = u32x2{pk_bf(gt * a0 + t[0], gt * a1 + t[1]), pk_bf(gt * a2 + t[2], gt * a3 + t[3])};
      }
    };
    if (kh == 0) finish(std::integral_constant<int, 0>{}, std::integral_constant<int, 3>{});
    else finish(std::integral_constant<int, 3>{}, std::integral_constant<int, 2>{});
    hw_barrier();
    const u32x4 r0 = lds128(sm, sr), r1 = lds128(sm, sr2);
    __builtin_amdgcn_raw_buffer_store_b128(r0, outr, oo, 0, 0);
    if (lane < 16) __builtin_amdgcn_raw_buffer_store_b128(r1, outr, oo2, 0, 0);
    STAMP(28);
  }
}

extern "C" void kernel_launch(void* const* d_in, const int* in_sizes, int n_in, void* d_out, int out_size, void* d_ws,
                              size_t ws_size, hipStream_t stream) {
  Params p;
  p.x = (const u16*)d_in[0];
  p.logA = (const u16*)d_in[1];
  p.Aim = (const u16*)d_in[2];
  p.Cre = (const u16*)d_in[3];
  p.Cim = (const u16*)d_in[4];
  p.logdt = (const u16*)d_in[5];
  p.Dsk = (const u16*)d_in[6];
  p.wq = (const u16*)d_in[8];
  p.wk = (const u16*)d_in[9];
  p.wv = (const u16*)d_in[10];
  p.wo = (const u16*)d_in[11];
  p.gw = (const u16*)d_in[12];
  p.out = (u16*)d_out;
  p.ws = (char*)d_ws;
  (void)hipFuncSetAttribute((const void*)fused_kernel, hipFuncAttributeMaxDynamicSharedMemorySize, LDS_BYTES);
  fused_kernel<<<256, 512, LDS_BYTES, stream>>>(p);
}
```
